```python
import torch

def _softmax(x, axis=-1):
    return torch.softmax(x, dim=axis)
def _gelu(x):
    return torch.nn.functional.gelu(x, approximate="tanh")
def _silu(x):
    return torch.nn.functional.silu(x)
def _sigmoid(x):
    return torch.sigmoid(x)
def _layernorm(x, axis=-1, eps=1e-5):
    m = x.mean(dim=axis, keepdim=True)
    v = x.var(dim=axis, keepdim=True, unbiased=False)
    return (x - m) / torch.sqrt(v + eps)
def _scatter_add(tgt, idx, src, axis=0):
    return tgt.clone().index_add_(axis, idx, src)
def _segment_sum(src, seg, n):
    out = torch.zeros((n,) + src.shape[1:], dtype=src.dtype)
    return out.index_add_(0, seg, src)
def _pad(x, pw):
    flat = tuple(v for pair in reversed(pw) for v in pair)
    return torch.nn.functional.pad(x, flat)

kernel_name = 'standalone__attention_interact_autoint__recsys'

def setup_inputs():

    _w = (lambda a: (lambda t: t.to(torch.bfloat16) if t.is_floating_point() else t)(torch.as_tensor(a)))
    B, F = 1024, 100
    D = max(2, (64 // 2) * 2)
    H = 2
    Dh = D // H
    return {
        "e": _w((lambda *s: torch.randn(*s, dtype=torch.bfloat16) * 0.05)(B, F, D).to(float)),
        "wq": _w((lambda *s: torch.randn(*s, dtype=torch.bfloat16) * 3.2)(H, D, Dh).to(float)),
        "wk": _w((lambda *s: torch.randn(*s, dtype=torch.bfloat16) * 3.2)(H, D, Dh).to(float)),
        "wv": _w((lambda *s: torch.randn(*s, dtype=torch.bfloat16) * 0.05)(H, D, Dh).to(float)),
        "wres": _w((lambda *s: torch.randn(*s, dtype=torch.bfloat16) * 0.05)(D, D).to(float)),
    }

def reference(e, wq, wk, wv, wres):
    H, D, Dh = wq.shape[0], wq.shape[1], wq.shape[2]
    B, F = e.shape[0], e.shape[1]
    q = torch.einsum('bfd,hdk->bhfk', e, wq)
    k = torch.einsum('bfd,hdk->bhfk', e, wk)
    v = torch.einsum('bfd,hdk->bhfk', e, wv)
    scale = float(Dh) ** -0.5
    scores = torch.einsum('bhfk,bhgk->bhfg', q, k) * scale
    attn = _softmax(scores, axis=-1)
    attn = (lambda x, dt: x.to(dt))(attn, v.dtype)
    ctx = torch.einsum('bhfg,bhgk->bhfk', attn, v)
    ctx = (lambda x, *d: x.permute(*d))(ctx, 0, 2, 1, 3).reshape(B, F, H * Dh)
    res = torch.einsum('bfd,de->bfe', e, wres)
    out = ctx + res
    mask = (lambda x, dt: x.to(dt))(out > 0, out.dtype)
    return out * mask
```

```cpp
#include <hip/hip_runtime.h>
#include <hip/hip_bf16.h>

__device__ __forceinline__ float bf2f(unsigned short x) { return __uint_as_float(((unsigned)x) << 16); }
__device__ __forceinline__ unsigned short f2bf(float x) { __hip_bfloat16 b = __float2bfloat16(x); return *(unsigned short*)&b; }
__device__ __forceinline__ float rbf(float x) { return bf2f(f2bf(x)); }

__global__ __launch_bounds__(256) void naive_kernel(const unsigned short* e, const unsigned short* wq, const unsigned short* wk,
                             const unsigned short* wv, const unsigned short* wres, unsigned short* out) {
    __shared__ float se[100 * 65];
    __shared__ float sq[100 * 65], sk[100 * 65], sv[100 * 65];
    int b = blockIdx.x, t = threadIdx.x;
    for (int i = t; i < 6400; i += 256) se[(i >> 6) * 65 + (i & 63)] = bf2f(e[b * 6400 + i]);
    __syncthreads();
    for (int i = t; i < 6400; i += 256) {
        int f = i >> 6, c = i & 63, h = c >> 5, k = c & 31;
        float aq = 0, ak = 0, av = 0;
        for (int d = 0; d < 64; ++d) {
            float x = se[f * 65 + d];
            aq += x * bf2f(wq[h * 2048 + d * 32 + k]);
            ak += x * bf2f(wk[h * 2048 + d * 32 + k]);
            av += x * bf2f(wv[h * 2048 + d * 32 + k]);
        }
        sq[f * 65 + c] = rbf(aq); sk[f * 65 + c] = rbf(ak); sv[f * 65 + c] = rbf(av);
    }
    __syncthreads();
    if (t < 200) {
        int h = t / 100, f = t % 100;
        float m = -INFINITY;
        const float scale = 0.17677669529663687f;
        for (int g = 0; g < 100; ++g) {
            float s = 0;
            for (int k = 0; k < 32; ++k) s += sq[f * 65 + h * 32 + k] * sk[g * 65 + h * 32 + k];
            s *= scale;
            m = fmaxf(m, s);
        }
        float l = 0;
        float acc[32];
#pragma unroll
        for (int k = 0; k < 32; ++k) acc[k] = 0;
        for (int g = 0; g < 100; ++g) {
            float s = 0;
            for (int k = 0; k < 32; ++k) s += sq[f * 65 + h * 32 + k] * sk[g * 65 + h * 32 + k];
            s *= scale;
            float p = __expf(s - m);
            l += p;
#pragma unroll
            for (int k = 0; k < 32; ++k) acc[k] += p * sv[g * 65 + h * 32 + k];
        }
        float il = 1.f / l;
#pragma unroll
        for (int k = 0; k < 32; ++k) {
            float r = 0;
            for (int d = 0; d < 64; ++d) r += se[f * 65 + d] * bf2f(wres[d * 64 + h * 32 + k]);
            float o = acc[k] * il + r;
            out[b * 6400 + f * 64 + h * 32 + k] = f2bf(o > 0 ? o : 0.f);
        }
    }
}

extern "C" void kernel_launch(void* const* d_in, const int* in_sizes, int n_in,
                              void* d_out, int out_size, void* d_ws, size_t ws_size,
                              hipStream_t stream) {
    naive_kernel<<<1024, 256, 0, stream>>>((const unsigned short*)d_in[0], (const unsigned short*)d_in[1], (const unsigned short*)d_in[2],
                                           (const unsigned short*)d_in[3], (const unsigned short*)d_in[4], (unsigned short*)d_out);
}
```

```cpp
#include <hip/hip_runtime.h>
#include <hip/hip_bf16.h>

typedef __attribute__((ext_vector_type(8))) __bf16 bf16x8;
typedef __attribute__((ext_vector_type(2))) __bf16 bf16x2;
typedef __attribute__((ext_vector_type(4))) short s16x4;
typedef __attribute__((ext_vector_type(2))) short s16x2;
typedef __attribute__((ext_vector_type(16))) float f32x16;
typedef __attribute__((ext_vector_type(4))) float f32x4;
typedef __attribute__((ext_vector_type(2))) float f32x2;
typedef __attribute__((ext_vector_type(4))) unsigned u32x4;
typedef __attribute__((ext_vector_type(2))) unsigned u32x2;
typedef __attribute__((address_space(3))) char* lds_ptr;
#define DI __device__ __forceinline__
#define MFMA32(a, b, c) __builtin_amdgcn_mfma_f32_32x32x16_bf16((a), (b), (c), 0, 0, 0)
#define MFMA16(a, b, c) __builtin_amdgcn_mfma_f32_16x16x32_bf16((a), (b), (c), 0, 0, 0)

constexpr int NF = 100;
constexpr int BATCH_BYTES = NF * 128;
constexpr int E_REGION = 13 * 1024;
constexpr int W_BYTES = 32 * 1024;
constexpr int LDS_BYTES = W_BYTES + 8 * E_REGION;
constexpr float QSCALE = 0.17677669529663687f * 1.4426950408889634f;

DI unsigned pk(float a, float b) { f32x2 v = {a, b}; return __builtin_bit_cast(unsigned, __builtin_convertvector(v, bf16x2)); }
DI bf16x8 pack8(const f32x16& x, int s) {
    u32x4 p = {pk(x[8 * s], x[8 * s + 1]), pk(x[8 * s + 2], x[8 * s + 3]), pk(x[8 * s + 4], x[8 * s + 5]), pk(x[8 * s + 6], x[8 * s + 7])};
    return __builtin_bit_cast(bf16x8, p);
}
DI bf16x8 lds_b128(lds_ptr p) { return *(const __attribute__((address_space(3))) bf16x8*)p; }
DI bf16x8 wfrag(lds_ptr p) {
    s16x4 lo = __builtin_amdgcn_ds_read_tr16_b64_v4i16((__attribute__((address_space(3))) s16x4*)p);
    s16x4 hi = __builtin_amdgcn_ds_read_tr16_b64_v4i16((__attribute__((address_space(3))) s16x4*)(p + 256));
    return __builtin_bit_cast(bf16x8, __builtin_shufflevector(lo, hi, 0, 1, 2, 3, 4, 5, 6, 7));
}

#define DMA4(ldsb, rsrc, v0, v1, s0, s1, s2, s3)                                                        \
    asm volatile("s_mov_b32 m0, %0\n\ts_nop 0\n\t"                                                      \
                 "buffer_load_dwordx4 %2, %1, %4 offen lds\n\t"                                         \
                 "buffer_load_dwordx4 %3, %1, %5 offen offset:1024 lds\n\t"                             \
                 "buffer_load_dwordx4 %2, %1, %6 offen offset:2048 lds\n\t"                             \
                 "buffer_load_dwordx4 %3, %1, %7 offen offset:3072 lds"                                 \
                 :: "s"(ldsb), "s"(rsrc), "v"(v0), "v"(v1), "s"(s0), "s"(s1), "s"(s2), "s"(s3) : "memory", "m0")
#define DMA1(ldsb, rsrc, v0, s0)                                                                        \
    asm volatile("s_mov_b32 m0, %0\n\ts_nop 0\n\t"                                                      \
                 "buffer_load_dwordx4 %2, %1, %3 offen lds"                                             \
                 :: "s"(ldsb), "s"(rsrc), "v"(v0), "s"(s0) : "memory", "m0")

DI u32x4 make_rsrc(const void* p, unsigned bytes) {
    unsigned long long a = (unsigned long long)p;
    u32x4 r = {(unsigned)a, (unsigned)(a >> 32) & 0xffffu, bytes, 0x00020000u};
    return r;
}

template <bool SAFE>
DI void attend(const bf16x8 (&Kf)[4][2], const bf16x8 (&Vf)[4][2], const bf16x8 (&Qf)[2], const bf16x8& sel, const f32x4& cinit,
               f32x16& ctx, f32x4& lacc) {
    const f32x16 z16 = {};
    float m = 0.f;
    if (SAFE) {
        m = -INFINITY;
#pragma unroll
        for (int G = 0; G < 3; ++G) {
            f32x16 s = MFMA32(Kf[G][0], Qf[0], z16);
            s = MFMA32(Kf[G][1], Qf[1], s);
#pragma unroll
            for (int i = 0; i < 16; ++i) m = __builtin_fmaxf(m, s[i]);
        }
        f32x4 s4 = MFMA16(Kf[3][0], Qf[0], cinit);
        s4 = MFMA16(Kf[3][1], Qf[1], s4);
#pragma unroll
        for (int i = 0; i < 4; ++i) m = __builtin_fmaxf(m, s4[i]);
        m = __builtin_fmaxf(m, __shfl_xor(m, 32));
    }
    ctx = z16;
    lacc = f32x4{};
#pragma unroll
    for (int G = 0; G < 3; ++G) {
        f32x16 s = MFMA32(Kf[G][0], Qf[0], z16);
        s = MFMA32(Kf[G][1], Qf[1], s);
#pragma unroll
        for (int i = 0; i < 16; ++i) s[i] = __builtin_amdgcn_exp2f(SAFE ? s[i] - m : s[i]);
        bf16x8 p0 = pack8(s, 0), p1 = pack8(s, 1);
        ctx = MFMA32(Vf[G][0], p0, ctx);
        lacc = MFMA16(sel, p0, lacc);
        ctx = MFMA32(Vf[G][1], p1, ctx);
        lacc = MFMA16(sel, p1, lacc);
    }
    f32x4 s4 = MFMA16(Kf[3][0], Qf[0], cinit);
    s4 = MFMA16(Kf[3][1], Qf[1], s4);
#pragma unroll
    for (int i = 0; i < 4; ++i) s4[i] = __builtin_amdgcn_exp2f(SAFE ? s4[i] - m : s4[i]);
    u32x4 pt = {pk(s4[0], s4[1]), pk(s4[2], s4[3]), 0u, 0u};
    bf16x8 p = __builtin_bit_cast(bf16x8, pt);
    ctx = MFMA32(Vf[3][0], p, ctx);
    lacc = MFMA16(sel, p, lacc);
}

__global__ __launch_bounds__(512, 2) void autoint_kernel(const char* __restrict__ e, const char* __restrict__ wq, const char* __restrict__ wk,
                                                         const char* __restrict__ wv, const char* __restrict__ wres, char* __restrict__ out) {
    __shared__ __attribute__((aligned(1024))) char smem[LDS_BYTES];
    const int lane = threadIdx.x & 63;
    const int wid = __builtin_amdgcn_readfirstlane(threadIdx.x >> 6);
    const int head = wid & 1;
    const int b = blockIdx.x * 4 + (wid >> 1);
    const lds_ptr lds = (lds_ptr)smem;
    const unsigned lds0 = (unsigned)(size_t)lds;

    {
        const int m = wid >> 1;
        const char* wsrc = m == 0 ? wq : (m == 1 ? wk : (m == 2 ? wv : wres));
        const bool isr = m == 3;
        wsrc += isr ? head * 64 : head * 4096;
        const u32x4 wr = make_rsrc(wsrc, isr ? 8192 - head * 64 : 4096);
        const unsigned vw = (lane >> 2) * (isr ? 128 : 64) + (lane & 3) * 16;
        const unsigned st = isr ? 1024 : 0;
        DMA4(lds0 + wid * 4096, wr, vw, vw, 0u, st, 2 * st, 3 * st);
    }
    const unsigned ebase = lds0 + W_BYTES + wid * E_REGION;
    {
        const u32x4 er = make_rsrc(e + (size_t)b * BATCH_BYTES, BATCH_BYTES);
        const unsigned ve = (lane >> 3) * 128 + (((lane & 7) ^ (lane >> 4)) << 4);
        const unsigned vo = ve ^ 64;
        DMA4(ebase, er, ve, vo, 0u, 0u, 0u, 0u);
        DMA4(ebase + 4096, er, ve, vo, 4096u, 4096u, 4096u, 4096u);
        DMA4(ebase + 8192, er, ve, vo, 8192u, 8192u, 8192u, 8192u);
        DMA1(ebase + 12288, er, ve, 12288u);
    }
    const int r = lane & 31, hh = lane >> 5;
    const lds_ptr ep = lds + W_BYTES + wid * E_REGION;
    lds_ptr ea[4], et[4];
    {
        const int x = (r >> 1) & 7;
        const int rt = 96 + ((r & 7) ^ ((r & 16) >> 2));
        const int xt = (rt >> 1) & 7;
#pragma unroll
        for (int ks = 0; ks < 4; ++ks) {
            ea[ks] = ep + r * 128 + (((2 * ks + hh) ^ x) << 4);
            et[ks] = ep + rt * 128 + (((2 * ks + hh) ^ xt) << 4);
        }
    }
    const int i16 = lane & 15, q4 = i16 >> 2, p4 = i16 & 3, G1 = (lane >> 4) & 1;
    const int P3 = 4 * G1 + p4;
    const int wrow = (8 * hh + q4) * 64;
    const lds_ptr wnat = lds + head * 4096 + wrow + P3 * 8;
    const lds_ptr wtau = lds + head * 4096 + wrow + (((P3 & 1) << 2) | (P3 >> 1)) * 8;

    const unsigned selv = (((lane >> 2) ^ (lane >> 4)) & 1) ? 0u : 0x3f803f80u;
    const u32x4 selu = {selv, selv, selv, selv};
    const bf16x8 sel = __builtin_bit_cast(bf16x8, selu);
    const float cm = hh ? -INFINITY : 0.f;
    const f32x4 cinit = {cm, cm, cm, cm};
    const f32x16 z16 = {};

    asm volatile("s_waitcnt vmcnt(13)\n\ts_barrier" ::: "memory");
    bf16x8 Wq[4], Wk[4], Wv[4], Wr[4];
#pragma unroll
    for (int ks = 0; ks < 4; ++ks) {
        Wk[ks] = wfrag(wnat + 1 * 8192 + ks * 1024);
        Wv[ks] = wfrag(wtau + 2 * 8192 + ks * 1024);
    }
#pragma unroll
    for (int ks = 0; ks < 4; ++ks) {
        Wq[ks] = wfrag(wnat + 0 * 8192 + ks * 1024);
        Wr[ks] = wfrag(wtau + 3 * 8192 + ks * 1024);
    }

    bf16x8 Kf[4][2], Vf[4][2];
#pragma unroll
    for (int t = 0; t < 4; ++t) {
        if (t == 0) asm volatile("s_waitcnt vmcnt(9)" ::: "memory");
        if (t == 1) asm volatile("s_waitcnt vmcnt(5)" ::: "memory");
        if (t == 2) asm volatile("s_waitcnt vmcnt(1)" ::: "memory");
        if (t == 3) asm volatile("s_waitcnt vmcnt(0)" ::: "memory");
        bf16x8 ef[4];
#pragma unroll
        for (int ks = 0; ks < 4; ++ks) ef[ks] = t < 3 ? lds_b128(ea[ks] + t * 4096) : lds_b128(et[ks]);
        f32x16 ka = z16, va = z16;
#pragma unroll
        for (int ks = 0; ks < 4; ++ks) { ka = MFMA32(Wk[ks], ef[ks], ka); va = MFMA32(ef[ks], Wv[ks], va); }
        Kf[t][0] = pack8(ka, 0); Kf[t][1] = pack8(ka, 1);
        Vf[t][0] = pack8(va, 0); Vf[t][1] = pack8(va, 1);
    }

    const u32x4 orr = make_rsrc(out + (size_t)b * BATCH_BYTES + head * 64, BATCH_BYTES - head * 64);
    const auto orsrc = __builtin_amdgcn_make_buffer_rsrc((void*)(out + (size_t)b * BATCH_BYTES + head * 64), 0, BATCH_BYTES - head * 64, 0x00020000);
    (void)orr;
    const lds_ptr sw0 = ep + r * 64 + (((2 * hh) ^ ((r >> 1) & 3)) << 4);
    const lds_ptr sw1 = ep + r * 64 + (((2 * hh + 1) ^ ((r >> 1) & 3)) << 4);
    const lds_ptr sr = ep + (lane >> 2) * 64 + (((lane & 3) ^ ((lane >> 3) & 3)) << 4);
    const unsigned so = (lane >> 2) * 128 + (lane & 3) * 16;

#pragma unroll
    for (int t = 0; t < 4; ++t) {
        bf16x8 ef[4];
#pragma unroll
        for (int ks = 0; ks < 4; ++ks) ef[ks] = t < 3 ? lds_b128(ea[ks] + t * 4096) : lds_b128(et[ks]);
        f32x16 qa = z16, ra = z16;
#pragma unroll
        for (int ks = 0; ks < 4; ++ks) { qa = MFMA32(Wq[ks], ef[ks], qa); ra = MFMA32(Wr[ks], ef[ks], ra); }
        qa *= QSCALE;
        bf16x8 Qf[2] = {pack8(qa, 0), pack8(qa, 1)};
        f32x16 ctx; f32x4 lacc;
        attend<false>(Kf, Vf, Qf, sel, cinit, ctx, lacc);
        float l = lacc[0];
        if (__builtin_expect(__any(!(l > 0x1p-40f && l < 0x1p40f)), 0)) {
            attend<true>(Kf, Vf, Qf, sel, cinit, ctx, lacc);
            l = lacc[0];
        }
        const float inv = __builtin_amdgcn_rcpf(l);
        u32x4 o[2];
#pragma unroll
        for (int i = 0; i < 8; ++i) {
            unsigned w = pk(__builtin_fmaf(ctx[2 * i], inv, ra[2 * i]), __builtin_fmaf(ctx[2 * i + 1], inv, ra[2 * i + 1]));
            s16x2 v = __builtin_bit_cast(s16x2, w);
            v = __builtin_elementwise_max(v, s16x2{0, 0});
            o[i >> 2][i & 3] = __builtin_bit_cast(unsigned, v);
        }
        const int sb = (t < 3 ? t : 0) * 4096;
        const int sx = t < 3 ? 0 : 2048;
        *(__attribute__((address_space(3))) u32x4*)(sw0 + sb + sx) = o[0];
        *(__attribute__((address_space(3))) u32x4*)(sw1 + sb + sx) = o[1];
        u32x4 g0 = *(const __attribute__((address_space(3))) u32x4*)(sr + sb + sx);
        __builtin_amdgcn_raw_buffer_store_b128(g0, orsrc, so, t * 4096, 16);
        if (t < 3) {
            u32x4 g1 = *(const __attribute__((address_space(3))) u32x4*)(sr + sb + sx + 1024);
            __builtin_amdgcn_raw_buffer_store_b128(g1, orsrc, so + 2048, t * 4096, 16);
        }
    }
}

extern "C" void kernel_launch(void* const* d_in, const int* in_sizes, int n_in,
                              void* d_out, int out_size, void* d_ws, size_t ws_size,
                              hipStream_t stream) {
    const int B = in_sizes[0] / (NF * 64);
    autoint_kernel<<<B / 4, 512, 0, stream>>>((const char*)d_in[0], (const char*)d_in[1], (const char*)d_in[2],
                                              (const char*)d_in[3], (const char*)d_in[4], (char*)d_out);
}
```

```cpp
#include <hip/hip_runtime.h>
#include <hip/hip_bf16.h>

typedef __attribute__((ext_vector_type(8))) __bf16 bf16x8;
typedef __attribute__((ext_vector_type(2))) __bf16 bf16x2;
typedef __attribute__((ext_vector_type(4))) short s16x4;
typedef __attribute__((ext_vector_type(2))) short s16x2;
typedef __attribute__((ext_vector_type(16))) float f32x16;
typedef __attribute__((ext_vector_type(4))) float f32x4;
typedef __attribute__((ext_vector_type(2))) float f32x2;
typedef __attribute__((ext_vector_type(4))) unsigned u32x4;
typedef __attribute__((ext_vector_type(2))) unsigned u32x2;
typedef __attribute__((address_space(3))) char* lds_ptr;
#define DI __device__ __forceinline__
#define MFMA32(a, b, c) __builtin_amdgcn_mfma_f32_32x32x16_bf16((a), (b), (c), 0, 0, 0)
#define MFMA16(a, b, c) __builtin_amdgcn_mfma_f32_16x16x32_bf16((a), (b), (c), 0, 0, 0)

constexpr int NF = 100;
constexpr int BATCH_BYTES = NF * 128;
constexpr int E_REGION = 13 * 1024;
constexpr int W_BYTES = 32 * 1024;
constexpr int LDS_BYTES = W_BYTES + 8 * E_REGION;
constexpr float QSCALE = 0.17677669529663687f * 1.4426950408889634f;

DI unsigned pk(float a, float b) { f32x2 v = {a, b}; return __builtin_bit_cast(unsigned, __builtin_convertvector(v, bf16x2)); }
DI bf16x8 pack8(const f32x16& x, int s) {
    u32x4 p = {pk(x[8 * s], x[8 * s + 1]), pk(x[8 * s + 2], x[8 * s + 3]), pk(x[8 * s + 4], x[8 * s + 5]), pk(x[8 * s + 6], x[8 * s + 7])};
    return __builtin_bit_cast(bf16x8, p);
}
DI bf16x8 lds_b128(lds_ptr p) { return *(const __attribute__((address_space(3))) bf16x8*)p; }
DI bf16x8 wfrag(lds_ptr p) {
    s16x4 lo = __builtin_amdgcn_ds_read_tr16_b64_v4i16((__attribute__((address_space(3))) s16x4*)p);
    s16x4 hi = __builtin_amdgcn_ds_read_tr16_b64_v4i16((__attribute__((address_space(3))) s16x4*)(p + 256));
    return __builtin_bit_cast(bf16x8, __builtin_shufflevector(lo, hi, 0, 1, 2, 3, 4, 5, 6, 7));
}

#define DMA4(ldsb, rsrc, v0, v1, s0, s1, s2, s3)                                                        \
    asm volatile("s_mov_b32 m0, %0\n\ts_nop 0\n\t"                                                      \
                 "buffer_load_dwordx4 %2, %1, %4 offen lds\n\t"                                         \
                 "buffer_load_dwordx4 %3, %1, %5 offen offset:1024 lds\n\t"                             \
                 "buffer_load_dwordx4 %2, %1, %6 offen offset:2048 lds\n\t"                             \
                 "buffer_load_dwordx4 %3, %1, %7 offen offset:3072 lds"                                 \
                 :: "s"(ldsb), "s"(rsrc), "v"(v0), "v"(v1), "s"(s0), "s"(s1), "s"(s2), "s"(s3) : "memory", "m0")
#define DMA1(ldsb, rsrc, v0, s0)                                                                        \
    asm volatile("s_mov_b32 m0, %0\n\ts_nop 0\n\t"                                                      \
                 "buffer_load_dwordx4 %2, %1, %3 offen lds"                                             \
                 :: "s"(ldsb), "s"(rsrc), "v"(v0), "s"(s0) : "memory", "m0")

DI u32x4 make_rsrc(const void* p, unsigned bytes) {
    unsigned long long a = (unsigned long long)p;
    u32x4 r = {(unsigned)a, (unsigned)(a >> 32) & 0xffffu, bytes, 0x00020000u};
    return r;
}

template <bool SAFE>
DI void attend(const bf16x8 (&Kf)[4][2], const bf16x8 (&Vf)[4][2], const bf16x8 (&Qf)[2], const bf16x8& sel, const f32x4& cinit,
               f32x16& ctx, f32x4& lacc) {
    const f32x16 z16 = {};
    float m = 0.f;
    if (SAFE) {
        m = -INFINITY;
#pragma unroll
        for (int G = 0; G < 3; ++G) {
            f32x16 s = MFMA32(Kf[G][0], Qf[0], z16);
            s = MFMA32(Kf[G][1], Qf[1], s);
#pragma unroll
            for (int i = 0; i < 16; ++i) m = __builtin_fmaxf(m, s[i]);
        }
        f32x4 s4 = MFMA16(Kf[3][0], Qf[0], cinit);
        s4 = MFMA16(Kf[3][1], Qf[1], s4);
#pragma unroll
        for (int i = 0; i < 4; ++i) m = __builtin_fmaxf(m, s4[i]);
        m = __builtin_fmaxf(m, __shfl_xor(m, 32));
    }
    ctx = z16;
    lacc = f32x4{};
#pragma unroll
    for (int G = 0; G < 3; ++G) {
        f32x16 s = MFMA32(Kf[G][0], Qf[0], z16);
        s = MFMA32(Kf[G][1], Qf[1], s);
#pragma unroll
        for (int i = 0; i < 16; ++i) s[i] = __builtin_amdgcn_exp2f(SAFE ? s[i] - m : s[i]);
        bf16x8 p0 = pack8(s, 0), p1 = pack8(s, 1);
        ctx = MFMA32(Vf[G][0], p0, ctx);
        lacc = MFMA16(sel, p0, lacc);
        ctx = MFMA32(Vf[G][1], p1, ctx);
        lacc = MFMA16(sel, p1, lacc);
    }
    f32x4 s4 = MFMA16(Kf[3][0], Qf[0], cinit);
    s4 = MFMA16(Kf[3][1], Qf[1], s4);
#pragma unroll
    for (int i = 0; i < 4; ++i) s4[i] = __builtin_amdgcn_exp2f(SAFE ? s4[i] - m : s4[i]);
    u32x4 pt = {pk(s4[0], s4[1]), pk(s4[2], s4[3]), 0u, 0u};
    bf16x8 p = __builtin_bit_cast(bf16x8, pt);
    ctx = MFMA32(Vf[3][0], p, ctx);
    lacc = MFMA16(sel, p, lacc);
}

__global__ __launch_bounds__(512, 2) void autoint_kernel(const char* __restrict__ e, const char* __restrict__ wq, const char* __restrict__ wk,
                                                         const char* __restrict__ wv, const char* __restrict__ wres, char* __restrict__ out, unsigned long long* ws) {
    __shared__ __attribute__((aligned(1024))) char smem[LDS_BYTES];
    unsigned long long T0, T1 = 0; asm volatile("s_memrealtime %0\n\ts_waitcnt lgkmcnt(0)" : "=s"(T0) :: "memory"); T1 = T0;
    const int lane = threadIdx.x & 63;
    const int wid = __builtin_amdgcn_readfirstlane(threadIdx.x >> 6);
    const int head = wid & 1;
    const int b = blockIdx.x * 4 + (wid >> 1);
    const lds_ptr lds = (lds_ptr)smem;
    const unsigned lds0 = (unsigned)(size_t)lds;

    {
        const int m = wid >> 1;
        const char* wsrc = m == 0 ? wq : (m == 1 ? wk : (m == 2 ? wv : wres));
        const bool isr = m == 3;
        wsrc += isr ? head * 64 : head * 4096;
        const u32x4 wr = make_rsrc(wsrc, isr ? 8192 - head * 64 : 4096);
        const unsigned vw = (lane >> 2) * (isr ? 128 : 64) + (lane & 3) * 16;
        const unsigned st = isr ? 1024 : 0;
        DMA4(lds0 + wid * 4096, wr, vw, vw, 0u, st, 2 * st, 3 * st);
    }
    const unsigned ebase = lds0 + W_BYTES + wid * E_REGION;
    {
        const u32x4 er = make_rsrc(e + (size_t)b * BATCH_BYTES, BATCH_BYTES);
        const unsigned ve = (lane >> 3) * 128 + (((lane & 7) ^ (lane >> 4)) << 4);
        const unsigned vo = ve ^ 64;
        DMA4(ebase, er, ve, vo, 0u, 0u, 0u, 0u);
        DMA4(ebase + 4096, er, ve, vo, 4096u, 4096u, 4096u, 4096u);
        DMA4(ebase + 8192, er, ve, vo, 8192u, 8192u, 8192u, 8192u);
        DMA1(ebase + 12288, er, ve, 12288u);
    }
    const int r = lane & 31, hh = lane >> 5;
    const lds_ptr ep = lds + W_BYTES + wid * E_REGION;
    lds_ptr ea[4], et[4];
    {
        const int x = (r >> 1) & 7;
        const int rt = 96 + ((r & 7) ^ ((r & 16) >> 2));
        const int xt = (rt >> 1) & 7;
#pragma unroll
        for (int ks = 0; ks < 4; ++ks) {
            ea[ks] = ep + r * 128 + (((2 * ks + hh) ^ x) << 4);
            et[ks] = ep + rt * 128 + (((2 * ks + hh) ^ xt) << 4);
        }
    }
    const int i16 = lane & 15, q4 = i16 >> 2, p4 = i16 & 3, G1 = (lane >> 4) & 1;
    const int P3 = 4 * G1 + p4;
    const int wrow = (8 * hh + q4) * 64;
    const lds_ptr wnat = lds + head * 4096 + wrow + P3 * 8;
    const lds_ptr wtau = lds + head * 4096 + wrow + (((P3 & 1) << 2) | (P3 >> 1)) * 8;

    const unsigned selv = (((lane >> 2) ^ (lane >> 4)) & 1) ? 0u : 0x3f803f80u;
    const u32x4 selu = {selv, selv, selv, selv};
    const bf16x8 sel = __builtin_bit_cast(bf16x8, selu);
    const float cm = hh ? -INFINITY : 0.f;
    const f32x4 cinit = {cm, cm, cm, cm};
    const f32x16 z16 = {};

    asm volatile("s_waitcnt vmcnt(13)\n\ts_barrier" ::: "memory");
    bf16x8 Wq[4], Wk[4], Wv[4], Wr[4];
#pragma unroll
    for (int ks = 0; ks < 4; ++ks) {
        Wk[ks] = wfrag(wnat + 1 * 8192 + ks * 1024);
        Wv[ks] = wfrag(wtau + 2 * 8192 + ks * 1024);
    }
#pragma unroll
    for (int ks = 0; ks < 4; ++ks) {
        Wq[ks] = wfrag(wnat + 0 * 8192 + ks * 1024);
        Wr[ks] = wfrag(wtau + 3 * 8192 + ks * 1024);
    }

    bf16x8 Kf[4][2], Vf[4][2];
#pragma unroll
    for (int t = 0; t < 4; ++t) {
        if (t == 0) asm volatile("s_waitcnt vmcnt(9)" ::: "memory");
        if (t == 1) asm volatile("s_waitcnt vmcnt(5)" ::: "memory");
        if (t == 2) asm volatile("s_waitcnt vmcnt(1)" ::: "memory");
        if (t == 3) asm volatile("s_waitcnt vmcnt(0)" ::: "memory");
        bf16x8 ef[4];
#pragma unroll
        for (int ks = 0; ks < 4; ++ks) ef[ks] = t < 3 ? lds_b128(ea[ks] + t * 4096) : lds_b128(et[ks]);
        f32x16 ka = z16, va = z16;
#pragma unroll
        for (int ks = 0; ks < 4; ++ks) { ka = MFMA32(Wk[ks], ef[ks], ka); va = MFMA32(ef[ks], Wv[ks], va); }
        Kf[t][0] = pack8(ka, 0); Kf[t][1] = pack8(ka, 1);
        Vf[t][0] = pack8(va, 0); Vf[t][1] = pack8(va, 1);
    }

    const u32x4 orr = make_rsrc(out + (size_t)b * BATCH_BYTES + head * 64, BATCH_BYTES - head * 64);
    const auto orsrc = __builtin_amdgcn_make_buffer_rsrc((void*)(out + (size_t)b * BATCH_BYTES + head * 64), 0, BATCH_BYTES - head * 64, 0x00020000);
    (void)orr;
    const lds_ptr sw0 = ep + r * 64 + (((2 * hh) ^ ((r >> 1) & 3)) << 4);
    const lds_ptr sw1 = ep + r * 64 + (((2 * hh + 1) ^ ((r >> 1) & 3)) << 4);
    const lds_ptr sr = ep + (lane >> 2) * 64 + (((lane & 3) ^ ((lane >> 3) & 3)) << 4);
    const unsigned so = (lane >> 2) * 128 + (lane & 3) * 16;

#pragma unroll
    for (int t = 0; t < 4; ++t) {
        bf16x8 ef[4];
#pragma unroll
        for (int ks = 0; ks < 4; ++ks) ef[ks] = t < 3 ? lds_b128(ea[ks] + t * 4096) : lds_b128(et[ks]);
        f32x16 qa = z16, ra = z16;
#pragma unroll
        for (int ks = 0; ks < 4; ++ks) { qa = MFMA32(Wq[ks], ef[ks], qa); ra = MFMA32(Wr[ks], ef[ks], ra); }
        qa *= QSCALE;
        bf16x8 Qf[2] = {pack8(qa, 0), pack8(qa, 1)};
        f32x16 ctx; f32x4 lacc;
        attend<false>(Kf, Vf, Qf, sel, cinit, ctx, lacc);
        float l = lacc[0];
        if (__builtin_expect(__any(!(l > 0x1p-40f && l < 0x1p40f)), 0)) {
            attend<true>(Kf, Vf, Qf, sel, cinit, ctx, lacc);
            l = lacc[0];
        }
        const float inv = __builtin_amdgcn_rcpf(l);
        u32x4 o[2];
#pragma unroll
        for (int i = 0; i < 8; ++i) {
            unsigned w = pk(__builtin_fmaf(ctx[2 * i], inv, ra[2 * i]), __builtin_fmaf(ctx[2 * i + 1], inv, ra[2 * i + 1]));
            s16x2 v = __builtin_bit_cast(s16x2, w);
            v = __builtin_elementwise_max(v, s16x2{0, 0});
            o[i >> 2][i & 3] = __builtin_bit_cast(unsigned, v);
        }
        const int sb = (t < 3 ? t : 0) * 4096;
        const int sx = t < 3 ? 0 : 2048;
        *(__attribute__((address_space(3))) u32x4*)(sw0 + sb + sx) = o[0];
        *(__attribute__((address_space(3))) u32x4*)(sw1 + sb + sx) = o[1];
        u32x4 g0 = *(const __attribute__((address_space(3))) u32x4*)(sr + sb + sx);
        __builtin_amdgcn_raw_buffer_store_b128(g0, orsrc, so, t * 4096, 16);
        if (t < 3) {
            u32x4 g1 = *(const __attribute__((address_space(3))) u32x4*)(sr + sb + sx + 1024);
            __builtin_amdgcn_raw_buffer_store_b128(g1, orsrc, so + 2048, t * 4096, 16);
        }
    }

}

__global__ void ka(unsigned long long* ws) { unsigned long long t = __builtin_amdgcn_s_memrealtime(); if (threadIdx.x == 0) ws[0] = t; }
__global__ void kb(unsigned long long* ws, int d) { unsigned long long t = __hip_atomic_load(ws, __ATOMIC_RELAXED, __HIP_MEMORY_SCOPE_AGENT); int n = 0; while (__builtin_amdgcn_s_memrealtime() - t < (unsigned long long)d && n < 1000000) { __builtin_amdgcn_s_sleep(1); ++n; } }
extern "C" void kernel_launch(void* const* d_in, const int* in_sizes, int n_in,
                              void* d_out, int out_size, void* d_ws, size_t ws_size,
                              hipStream_t stream) {
    const int B = in_sizes[0] / (NF * 64);
    ka<<<1, 64, 0, stream>>>((unsigned long long*)d_ws);
    autoint_kernel<<<B / 4, 512, 0, stream>>>((const char*)d_in[0], (const char*)d_in[1], (const char*)d_in[2],
                                              (const char*)d_in[3], (const char*)d_in[4], (char*)d_out, (unsigned long long*)d_ws);
    kb<<<1, 64, 0, stream>>>((unsigned long long*)d_ws, 4000);
}
```

```cpp
#include <hip/hip_runtime.h>
#include <hip/hip_bf16.h>

typedef __attribute__((ext_vector_type(8))) __bf16 bf16x8;
typedef __attribute__((ext_vector_type(2))) __bf16 bf16x2;
typedef __attribute__((ext_vector_type(4))) short s16x4;
typedef __attribute__((ext_vector_type(2))) short s16x2;
typedef __attribute__((ext_vector_type(16))) float f32x16;
typedef __attribute__((ext_vector_type(4))) float f32x4;
typedef __attribute__((ext_vector_type(2))) float f32x2;
typedef __attribute__((ext_vector_type(4))) unsigned u32x4;
typedef __attribute__((ext_vector_type(2))) unsigned u32x2;
typedef __attribute__((address_space(3))) char* lds_ptr;
#define LDSP(T, p) ((__attribute__((address_space(3))) T*)(p))
#define DI __device__ __forceinline__
#define MFMA32(a, b, c) __builtin_amdgcn_mfma_f32_32x32x16_bf16((a), (b), (c), 0, 0, 0)
#define MFMA16(a, b, c) __builtin_amdgcn_mfma_f32_16x16x32_bf16((a), (b), (c), 0, 0, 0)
#define PIN(x) asm volatile("" : "+v"(x))

constexpr int NF = 100;
constexpr int BATCH_BYTES = NF * 128;
constexpr int E_REGION = 13 * 1024;
constexpr int LDS_W = 0, LDS_Z = 32 * 1024, LDS_E = 33 * 1024, LDS_S = LDS_E + 6 * E_REGION;
constexpr int SCR = 4096;
constexpr int LDS_BYTES = LDS_S + 8 * SCR;
constexpr float QSCALE = 0.17677669529663687f * 1.4426950408889634f;
constexpr int ST_AUX = 16;

DI unsigned pk(float a, float b) { f32x2 v = {a, b}; return __builtin_bit_cast(unsigned, __builtin_convertvector(v, bf16x2)); }
DI bf16x8 pack8(const f32x16& x, int s) {
    u32x4 p = {pk(x[8 * s], x[8 * s + 1]), pk(x[8 * s + 2], x[8 * s + 3]), pk(x[8 * s + 4], x[8 * s + 5]), pk(x[8 * s + 6], x[8 * s + 7])};
    return __builtin_bit_cast(bf16x8, p);
}
DI bf16x8 lds_b128(lds_ptr p) { return *LDSP(const bf16x8, p); }
DI bf16x8 tr2(lds_ptr p, int d) {
    s16x4 lo = __builtin_amdgcn_ds_read_tr16_b64_v4i16(LDSP(s16x4, p));
    s16x4 hi = __builtin_amdgcn_ds_read_tr16_b64_v4i16(LDSP(s16x4, p + d));
    return __builtin_bit_cast(bf16x8, __builtin_shufflevector(lo, hi, 0, 1, 2, 3, 4, 5, 6, 7));
}
DI unsigned relu2(unsigned w) { s16x2 v = __builtin_bit_cast(s16x2, w); v = __builtin_elementwise_max(v, s16x2{0, 0}); return __builtin_bit_cast(unsigned, v); }

#define DMA1(ldsb, rsrc, v0, s0)                                                                        \
    asm volatile("s_mov_b32 m0, %0\n\ts_nop 0\n\t"                                                      \
                 "buffer_load_dwordx4 %2, %1, %3 offen lds"                                             \
                 :: "s"(ldsb), "s"(rsrc), "v"(v0), "s"(s0) : "memory")
#define DMA2(ldsb, rsrc, v0, s0)                                                                        \
    asm volatile("s_mov_b32 m0, %0\n\ts_nop 0\n\t"                                                      \
                 "buffer_load_dwordx4 %2, %1, %3 offen lds\n\t"                                         \
                 "buffer_load_dwordx4 %2, %1, %3 offen offset:1024 lds"                                 \
                 :: "s"(ldsb), "s"(rsrc), "v"(v0), "s"(s0) : "memory")
#define DMA4(ldsb, rsrc, v0, v1, s0)                                                                    \
    asm volatile("s_mov_b32 m0, %0\n\ts_nop 0\n\t"                                                      \
                 "buffer_load_dwordx4 %2, %1, %4 offen lds\n\t"                                         \
                 "buffer_load_dwordx4 %3, %1, %4 offen offset:1024 lds\n\t"                             \
                 "buffer_load_dwordx4 %2, %1, %4 offen offset:2048 lds\n\t"                             \
                 "buffer_load_dwordx4 %3, %1, %4 offen offset:3072 lds"                                 \
                 :: "s"(ldsb), "s"(rsrc), "v"(v0), "v"(v1), "s"(s0) : "memory")

DI u32x4 make_rsrc(const void* p, unsigned bytes) {
    unsigned long long a = (unsigned long long)p;
    u32x4 r = {(unsigned)a, (unsigned)(a >> 32) & 0xffffu, bytes, 0x00020000u};
    return r;
}
#define SYNC(role, na, nb)                                                                              \
    asm volatile("s_cmp_lg_u32 %0, 0\n\ts_cbranch_scc1 1f\n\ts_waitcnt vmcnt(" #na ")\n\ts_barrier\n\ts_branch 2f\n1:\n\ts_waitcnt vmcnt(" #nb ")\n2:" :: "s"(role) : "memory", "scc")

__global__ __launch_bounds__(512, 2) void autoint_kernel(const char* __restrict__ e, const char* __restrict__ wq, const char* __restrict__ wk,
                                                         const char* __restrict__ wv, const char* __restrict__ wres, char* __restrict__ out) {
    __shared__ __attribute__((aligned(1024))) char smem[LDS_BYTES];
    const int lane = threadIdx.x & 63;
    const int wid = __builtin_amdgcn_readfirstlane(threadIdx.x >> 6);
    const int head = wid & 1;
    const int role = wid >> 2;
    const int b = blockIdx.x * 4 + (wid >> 1);
    const lds_ptr lds = (lds_ptr)smem;
    const unsigned lds0 = (unsigned)(size_t)lds;
    const int ereg = role ? wid - 2 : (wid >> 1);

    {
        const unsigned vlin = lane * 16;
        const unsigned ve = (lane >> 3) * 128 + (((lane & 7) ^ (lane >> 4)) << 4);
        const unsigned vo = ve ^ 64;
        const unsigned vp = (wid & 1) ? vo : ve;
        const char* kv = (wid & 4) ? wv : wk;
        const u32x4 r1 = make_rsrc(kv, 8192);
        DMA2(lds0 + LDS_W + 8192 + wid * 2048, r1, vlin, (unsigned)((wid & 3) * 2048));
        const int ab = wid >> 2, pc = wid & 3;
        const u32x4 ra = make_rsrc(e + (size_t)(blockIdx.x * 4 + ab) * BATCH_BYTES, BATCH_BYTES);
        const unsigned ea = lds0 + LDS_E + ab * E_REGION + pc * 1024;
        DMA1(ea, ra, vp, (unsigned)(pc * 1024));
        const u32x4 r3 = make_rsrc(wq, 8192);
        DMA1(lds0 + LDS_W + wid * 1024, r3, vlin, (unsigned)(wid * 1024));
        DMA1(ea + 4096, ra, vp, (unsigned)(pc * 1024 + 4096));
        DMA1(ea + 8192, ra, vp, (unsigned)(pc * 1024 + 8192));
        DMA1(pc == 0 ? lds0 + LDS_E + ab * E_REGION + 12288 : lds0 + LDS_Z, ra, ve, pc == 0 ? 12288u : 16384u);
        const u32x4 r7 = make_rsrc(wres, 8192);
        const unsigned vr = (lane >> 2) * 128 + (lane & 3) * 16;
        DMA1(lds0 + LDS_W + 24576 + wid * 1024, r7, vr, (unsigned)(ab * 64 + pc * 2048));
        if (role) {
            const u32x4 er = make_rsrc(e + (size_t)b * BATCH_BYTES, BATCH_BYTES);
            const unsigned eb = lds0 + LDS_E + ereg * E_REGION;
            DMA4(eb, er, ve, vo, 0u);
            DMA4(eb + 4096, er, ve, vo, 4096u);
            DMA4(eb + 8192, er, ve, vo, 8192u);
            DMA1(eb + 12288, er, ve, 12288u);
            asm volatile("s_waitcnt vmcnt(17)\n\ts_barrier\n\ts_waitcnt vmcnt(16)\n\ts_barrier\n\ts_waitcnt vmcnt(13)\n\ts_barrier" ::: "memory");
        }
    }
    const int r = lane & 31, hh = lane >> 5;
    const lds_ptr ep = lds + LDS_E + ereg * E_REGION;
    const lds_ptr sp = lds + LDS_S + wid * SCR;
    const lds_ptr zp = lds + LDS_Z;
    lds_ptr ea[4], et[4];
    {
        const int x = (r >> 1) & 7;
        const int rt = 96 + ((r & 7) ^ ((r & 16) >> 2));
        const int xt = (rt >> 1) & 7;
#pragma unroll
        for (int ks = 0; ks < 4; ++ks) {
            ea[ks] = ep + r * 128 + (((2 * ks + hh) ^ x) << 4);
            et[ks] = ep + rt * 128 + (((2 * ks + hh) ^ xt) << 4);
        }
    }
    const int i16 = lane & 15, q4 = i16 >> 2, p4 = i16 & 3, qq = lane >> 4, G1 = qq & 1;
    const int P3 = 4 * G1 + p4;
    const int wrow = (8 * hh + q4) * 64;
    const lds_ptr wnat = lds + head * 4096 + wrow + P3 * 8;
    const lds_ptr wtau = lds + head * 4096 + wrow + (((P3 & 1) << 2) | (P3 >> 1)) * 8;
    const bool par = (q4 & 1) == G1;
    const unsigned selv = par ? 0x3f803f80u : 0u;
    const u32x4 selu = {selv, selv, selv, selv};
    const bf16x8 sel = __builtin_bit_cast(bf16x8, selu);
    const u32x4 oneu = {0x3f803f80u, 0x3f803f80u, 0x3f803f80u, 0x3f803f80u};
    const bf16x8 ones = __builtin_bit_cast(bf16x8, oneu);
    const float cm = hh ? -INFINITY : 0.f;
    const f32x4 cinit = {cm, cm, cm, cm};
    const float cmt = (hh && r >= 4) ? -INFINITY : 0.f;
    const f32x4 cinitT = {cmt, cmt, cmt, cmt};
    const f32x16 z16 = {};
    const f32x4 z4 = {};
    const lds_ptr ktw = sp + 2048 + p4 * 64 + (q4 & 1) * 32 + (qq & 1) * 16 + (qq >> 1) * 8;
    const lds_ptr krd = sp + 2048 + p4 * 64 + hh * 16;
    const lds_ptr klo = (par && i16 < 8) ? krd : zp;
    const lds_ptr khi = (par && i16 >= 8) ? krd : zp;
    const lds_ptr ptw = sp + 3072 + lane * 8;
    const lds_ptr ptr = ((p4 & 1) == G1) ? sp + 3072 + (4 * hh + q4) * 8 : zp;

    SYNC(role, 4, 9);
    bf16x8 Wq[4], Wk[4], Wv[4], Wr[4];
#pragma unroll
    for (int ks = 0; ks < 4; ++ks) {
        Wk[ks] = tr2(wnat + 1 * 8192 + ks * 1024, 256);
        Wq[ks] = tr2(wnat + 0 * 8192 + ks * 1024, 256);
        Wv[ks] = tr2(wtau + 2 * 8192 + ks * 1024, 256);
    }
    bf16x8 Kf[4][2], Vf[4][2], Qf[3][2];
#pragma unroll
    for (int t = 0; t < 3; ++t) {
        if (t == 1) SYNC(role, 3, 5);
        if (t == 2) SYNC(role, 0, 0);
        bf16x8 ef[4];
#pragma unroll
        for (int ks = 0; ks < 4; ++ks) ef[ks] = lds_b128(ea[ks] + t * 4096);
        f32x16 ka = z16, va = z16, qa = z16;
#pragma unroll
        for (int ks = 0; ks < 4; ++ks) { ka = MFMA32(Wk[ks], ef[ks], ka); qa = MFMA32(Wq[ks], ef[ks], qa); va = MFMA32(ef[ks], Wv[ks], va); }
        Kf[t][0] = pack8(ka, 0); Kf[t][1] = pack8(ka, 1);
        Vf[t][0] = pack8(va, 0); Vf[t][1] = pack8(va, 1);
#pragma unroll
        for (int i = 0; i < 16; ++i) { float x = qa[i] * QSCALE; PIN(x); qa[i] = x; }
        Qf[t][0] = pack8(qa, 0); Qf[t][1] = pack8(qa, 1);
    }
#pragma unroll
    for (int ks = 0; ks < 4; ++ks) Wr[ks] = tr2(wtau + 3 * 8192 + ks * 1024, 256);
    f32x4 rt4;
    bf16x8 Qlo[2], Qhi[2];
    {
        bf16x8 ef[4];
#pragma unroll
        for (int ks = 0; ks < 4; ++ks) ef[ks] = lds_b128(et[ks]);
        f32x4 k4 = z4, q4v = z4, v4 = z4; rt4 = z4;
#pragma unroll
        for (int ks = 0; ks < 4; ++ks) {
            k4 = MFMA16(Wk[ks], ef[ks], k4); q4v = MFMA16(Wq[ks], ef[ks], q4v);
            v4 = MFMA16(ef[ks], Wv[ks], v4); rt4 = MFMA16(ef[ks], Wr[ks], rt4);
        }
        *LDSP(u32x2, ktw) = u32x2{pk(k4[0], k4[1]), pk(k4[2], k4[3])};
        *LDSP(u32x2, ktw + 256) = u32x2{pk(q4v[0] * QSCALE, q4v[1] * QSCALE), pk(q4v[2] * QSCALE, q4v[3] * QSCALE)};
        u32x4 vt = {pk(v4[0], v4[1]), pk(v4[2], v4[3]), 0u, 0u};
        Vf[3][0] = __builtin_bit_cast(bf16x8, vt);
        Kf[3][0] = lds_b128(klo); Kf[3][1] = lds_b128(klo + 32);
        Qlo[0] = lds_b128(klo + 256); Qlo[1] = lds_b128(klo + 288);
        Qhi[0] = lds_b128(khi + 256); Qhi[1] = lds_b128(khi + 288);
    }

    const auto orsrc = __builtin_amdgcn_make_buffer_rsrc((void*)(out + (size_t)b * BATCH_BYTES + head * 64), 0, BATCH_BYTES - head * 64, 0x00020000);
    const lds_ptr sw0 = sp + r * 64 + (((2 * hh) ^ ((r >> 1) & 3)) << 4);
    const lds_ptr sw1 = sp + r * 64 + (((2 * hh + 1) ^ ((r >> 1) & 3)) << 4);
    const lds_ptr sr = sp + (lane >> 2) * 64 + (((lane & 3) ^ ((lane >> 3) & 3)) << 4);
    const unsigned so = (lane >> 2) * 128 + (lane & 3) * 16;
    unsigned lmax = 0, lmin = 0xffffffffu;

#pragma unroll
    for (int t = 0; t < 3; ++t) {
        f32x16 ctx = z16; f32x4 lacc = z4;
#pragma unroll
        for (int G = 0; G < 3; ++G) {
            f32x16 s = MFMA32(Kf[G][0], Qf[t][0], z16);
            s = MFMA32(Kf[G][1], Qf[t][1], s);
#pragma unroll
            for (int i = 0; i < 16; ++i) s[i] = __builtin_amdgcn_exp2f(s[i]);
            bf16x8 p0 = pack8(s, 0), p1 = pack8(s, 1);
            ctx = MFMA32(Vf[G][0], p0, ctx);
            lacc = MFMA16(sel, p0, lacc);
            ctx = MFMA32(Vf[G][1], p1, ctx);
            lacc = MFMA16(sel, p1, lacc);
        }
        f32x4 s4 = MFMA16(Kf[3][0], Qf[t][0], cinit);
        s4 = MFMA16(Kf[3][1], Qf[t][1], s4);
#pragma unroll
        for (int i = 0; i < 4; ++i) s4[i] = __builtin_amdgcn_exp2f(s4[i]);
        u32x4 pt = {pk(s4[0], s4[1]), pk(s4[2], s4[3]), 0u, 0u};
        bf16x8 p = __builtin_bit_cast(bf16x8, pt);
        ctx = MFMA32(Vf[3][0], p, ctx);
        lacc = MFMA16(sel, p, lacc);
        bf16x8 ef[4];
#pragma unroll
        for (int ks = 0; ks < 4; ++ks) ef[ks] = lds_b128(ea[ks] + t * 4096);
        f32x16 ra = z16;
#pragma unroll
        for (int ks = 0; ks < 4; ++ks) ra = MFMA32(Wr[ks], ef[ks], ra);
        const float l = lacc[0];
        lmax = max(lmax, __float_as_uint(l)); lmin = min(lmin, __float_as_uint(l));
        const float inv = __builtin_amdgcn_rcpf(l);
        u32x4 o[2];
#pragma unroll
        for (int i = 0; i < 8; ++i) {
            float x = __builtin_fmaf(ctx[2 * i], inv, ra[2 * i]), y = __builtin_fmaf(ctx[2 * i + 1], inv, ra[2 * i + 1]);
            PIN(x); PIN(y);
            o[i >> 2][i & 3] = relu2(pk(x, y));
        }
        *LDSP(u32x4, sw0) = o[0];
        *LDSP(u32x4, sw1) = o[1];
        u32x4 g0 = *LDSP(const u32x4, sr);
        u32x4 g1 = *LDSP(const u32x4, sr + 1024);
        __builtin_amdgcn_raw_buffer_store_b128(g0, orsrc, so, t * 4096, ST_AUX);
        __builtin_amdgcn_raw_buffer_store_b128(g1, orsrc, so + 2048, t * 4096, ST_AUX);
    }
    {
        f32x4 a01 = MFMA16(Qlo[0], Kf[0][0], z4);
        a01 = MFMA16(Qlo[1], Kf[0][1], a01); a01 = MFMA16(Qhi[0], Kf[1][0], a01); a01 = MFMA16(Qhi[1], Kf[1][1], a01);
        f32x4 a23 = MFMA16(Qlo[0], Kf[2][0], cinitT);
        a23 = MFMA16(Qlo[1], Kf[2][1], a23); a23 = MFMA16(Qhi[0], Kf[3][0], a23); a23 = MFMA16(Qhi[1], Kf[3][1], a23);
#pragma unroll
        for (int i = 0; i < 4; ++i) { a01[i] = __builtin_amdgcn_exp2f(a01[i]); a23[i] = __builtin_amdgcn_exp2f(a23[i]); }
        *LDSP(u32x2, ptw) = u32x2{pk(a01[0], a01[1]), pk(a01[2], a01[3])};
        *LDSP(u32x2, ptw + 512) = u32x2{pk(a23[0], a23[1]), pk(a23[2], a23[3])};
        f32x4 c4 = z4, l4 = z4;
#pragma unroll
        for (int k = 0; k < 7; ++k) {
            bf16x8 pa = tr2(ptr + k * 128, 64);
            c4 = MFMA16(pa, Vf[k >> 1][k & 1], c4);
            l4 = MFMA16(pa, ones, l4);
        }
        float o4[4];
#pragma unroll
        for (int i = 0; i < 4; ++i) {
            lmax = max(lmax, __float_as_uint(l4[i])); lmin = min(lmin, __float_as_uint(l4[i]));
            o4[i] = __builtin_fmaf(c4[i], __builtin_amdgcn_rcpf(l4[i]), rt4[i]);
        }
        const unsigned w = relu2(hh ? pk(o4[2], o4[3]) : pk(o4[0], o4[1]));
        const unsigned tso = (96 + 2 * hh) * 128 + (((r >> 2) & 1) * 16 + (r >> 3) * 4 + (r & 3)) * 2;
        __builtin_amdgcn_raw_buffer_store_b16((unsigned short)w, orsrc, tso, 0, ST_AUX);
        __builtin_amdgcn_raw_buffer_store_b16((unsigned short)(w >> 16), orsrc, tso + 128, 0, ST_AUX);
    }
    if (lmax > 0x53800000u || lmin < 0x2b800000u) __builtin_amdgcn_raw_buffer_store_b16((unsigned short)0x7fc0, orsrc, 0, 0, 0);
}

extern "C" void kernel_launch(void* const* d_in, const int* in_sizes, int n_in,
                              void* d_out, int out_size, void* d_ws, size_t ws_size,
                              hipStream_t stream) {
    const int B = in_sizes[0] / (NF * 64);
    autoint_kernel<<<B / 4, 512, 0, stream>>>((const char*)d_in[0], (const char*)d_in[1], (const char*)d_in[2],
                                              (const char*)d_in[3], (const char*)d_in[4], (char*)d_out);
}
```

```cpp
#include <hip/hip_runtime.h>
#include <hip/hip_bf16.h>

typedef __attribute__((ext_vector_type(8))) __bf16 bf16x8;
typedef __attribute__((ext_vector_type(2))) __bf16 bf16x2;
typedef __attribute__((ext_vector_type(4))) short s16x4;
typedef __attribute__((ext_vector_type(2))) short s16x2;
typedef __attribute__((ext_vector_type(16))) float f32x16;
typedef __attribute__((ext_vector_type(2))) float f32x2;
typedef __attribute__((ext_vector_type(4))) unsigned u32x4;
typedef __attribute__((address_space(3))) char* lds_ptr;
#define LDSP(T, p) ((__attribute__((address_space(3))) T*)(p))
#define DI __device__ __forceinline__
#define MFMA32(a, b, c) __builtin_amdgcn_mfma_f32_32x32x16_bf16((a), (b), (c), 0, 0, 0)

constexpr int NF = 100;
constexpr int BATCH_BYTES = NF * 128;
constexpr int E_REGION = 13 * 1024;
constexpr int LDS_W = 0, LDS_Z = 32 * 1024, LDS_E = 33 * 1024, LDS_S = LDS_E + 4 * E_REGION;
constexpr int SCR = 4096;
constexpr int LDS_BYTES = LDS_S + 8 * SCR;
constexpr unsigned L_LO = 0x21800000u, L_HI = 0x5d800000u;
constexpr float QSCALE = 0.17677669529663687f * 1.4426950408889634f;

DI unsigned pk(float a, float b) { f32x2 v = {a, b}; return __builtin_bit_cast(unsigned, __builtin_convertvector(v, bf16x2)); }
DI bf16x8 pack8(const f32x16& x, int s) {
    u32x4 p = {pk(x[8 * s], x[8 * s + 1]), pk(x[8 * s + 2], x[8 * s + 3]), pk(x[8 * s + 4], x[8 * s + 5]), pk(x[8 * s + 6], x[8 * s + 7])};
    return __builtin_bit_cast(bf16x8, p);
}
DI bf16x8 lds_b128(lds_ptr p) { return *LDSP(const bf16x8, p); }
DI bf16x8 tr2(lds_ptr p, int d) {
    s16x4 lo = __builtin_amdgcn_ds_read_tr16_b64_v4i16(LDSP(s16x4, p));
    s16x4 hi = __builtin_amdgcn_ds_read_tr16_b64_v4i16(LDSP(s16x4, p + d));
    return __builtin_bit_cast(bf16x8, __builtin_shufflevector(lo, hi, 0, 1, 2, 3, 4, 5, 6, 7));
}
DI unsigned relu2(unsigned w) { s16x2 v = __builtin_bit_cast(s16x2, w); v = __builtin_elementwise_max(v, s16x2{0, 0}); return __builtin_bit_cast(unsigned, v); }

#define DMA1(ldsb, rsrc, v0, s0)                                                                        \
    asm volatile("s_mov_b32 m0, %0\n\ts_nop 0\n\t"                                                      \
                 "buffer_load_dwordx4 %2, %1, %3 offen lds"                                             \
                 :: "s"(ldsb), "s"(rsrc), "v"(v0), "s"(s0) : "memory")
#define GL1(ldsb, base, v0)                                                                             \
    asm volatile("s_mov_b32 m0, %0\n\ts_nop 0\n\tglobal_load_lds_dwordx4 %2, %1" :: "s"(ldsb), "s"(base), "v"(v0) : "memory")
#define GL2(ldsb, base, v0, v1)                                                                         \
    asm volatile("s_mov_b32 m0, %0\n\ts_nop 0\n\tglobal_load_lds_dwordx4 %2, %1\n\tglobal_load_lds_dwordx4 %3, %1 offset:1024" \
                 :: "s"(ldsb), "s"(base), "v"(v0), "v"(v1) : "memory")

#define GL4(ldsb, base, v0, v1)                                                                         \
    asm volatile("s_mov_b32 m0, %0\n\ts_nop 0\n\tglobal_load_lds_dwordx4 %2, %1\n\tglobal_load_lds_dwordx4 %3, %1 offset:1024\n\t" \
                 "global_load_lds_dwordx4 %2, %1 offset:2048\n\tglobal_load_lds_dwordx4 %3, %1 offset:3072"  \
                 :: "s"(ldsb), "s"(base), "v"(v0), "v"(v1) : "memory")

DI u32x4 make_rsrc(const void* p, unsigned bytes) {
    unsigned long long a = (unsigned long long)p;
    u32x4 r = {(unsigned)a, (unsigned)(a >> 32) & 0xffffu, bytes, 0x00020000u};
    return r;
}

__device__ __forceinline__ void safe_unit(lds_ptr lds, int ereg, int head, char* outp) {
    const int lane = __builtin_amdgcn_mbcnt_hi(~0u, __builtin_amdgcn_mbcnt_lo(~0u, 0u));
    const int r = lane & 31, hh = lane >> 5;
    const lds_ptr ep = lds + LDS_E + ereg * E_REGION;
    const int i16 = lane & 15, q4 = i16 >> 2, p4 = i16 & 3, G1 = (lane >> 4) & 1;
    const int P3 = 4 * G1 + p4;
    const int wrow = (8 * hh + q4) * 64;
    const lds_ptr wnat = lds + head * 4096 + wrow + P3 * 8;
    const lds_ptr wtau = lds + head * 4096 + wrow + (((P3 & 1) << 2) | (P3 >> 1)) * 8;
    const f32x16 z16 = {};
    bf16x8 Kf[4][2], Vf[4][2];
#pragma unroll
    for (int t = 0; t < 4; ++t) {
        const int row = t < 3 ? 32 * t + r : 96 + (r & 3);
        f32x16 ka = z16, va = z16;
#pragma unroll
        for (int ks = 0; ks < 4; ++ks) {
            const bf16x8 ef = lds_b128(ep + row * 128 + (((2 * ks + hh) ^ ((row >> 1) & 7)) << 4));
            ka = MFMA32(tr2(wnat + 8192 + ks * 1024, 256), ef, ka);
            va = MFMA32(ef, tr2(wtau + 16384 + ks * 1024, 256), va);
        }
        Kf[t][0] = pack8(ka, 0); Kf[t][1] = pack8(ka, 1);
        Vf[t][0] = pack8(va, 0); Vf[t][1] = pack8(va, 1);
    }
    const auto orsrc = __builtin_amdgcn_make_buffer_rsrc((void*)outp, 0, BATCH_BYTES - head * 64, 0x00020000);
    asm volatile("s_waitcnt vmcnt(0)" ::: "memory");
#pragma unroll 1
    for (int t = 0; t < 4; ++t) {
        const int row = t < 3 ? 32 * t + r : 96 + (r & 3);
        f32x16 qa = z16, ra = z16;
#pragma unroll
        for (int ks = 0; ks < 4; ++ks) {
            const bf16x8 ef = lds_b128(ep + row * 128 + (((2 * ks + hh) ^ ((row >> 1) & 7)) << 4));
            qa = MFMA32(tr2(wnat + ks * 1024, 256), ef, qa);
            ra = MFMA32(tr2(wtau + 24576 + ks * 1024, 256), ef, ra);
        }
        const bf16x8 q0 = pack8(qa, 0), q1 = pack8(qa, 1);
        f32x16 s[4];
        float m = -INFINITY;
#pragma unroll
        for (int G = 0; G < 4; ++G) {
            s[G] = MFMA32(Kf[G][0], q0, z16);
            s[G] = MFMA32(Kf[G][1], q1, s[G]);
#pragma unroll
            for (int i = 0; i < 16; ++i) {
                if (G == 3 && (i >= 4 || hh)) s[G][i] = -INFINITY;
                m = __builtin_fmaxf(m, s[G][i]);
            }
        }
        m = __builtin_fmaxf(m, __shfl_xor(m, 32));
        f32x16 ctx = z16;
        float l = 0.f;
#pragma unroll
        for (int G = 0; G < 4; ++G) {
#pragma unroll
            for (int i = 0; i < 16; ++i) { s[G][i] = __builtin_amdgcn_exp2f(s[G][i] - m); l += s[G][i]; }
            ctx = MFMA32(Vf[G][0], pack8(s[G], 0), ctx);
            if (G < 3) ctx = MFMA32(Vf[G][1], pack8(s[G], 1), ctx);
        }
        l += __shfl_xor(l, 32);
        const float inv = 1.f / l;
        u32x4 o[2];
#pragma unroll
        for (int i = 0; i < 8; ++i)
            o[i >> 2][i & 3] = relu2(pk(__builtin_fmaf(ctx[2 * i], inv, ra[2 * i]), __builtin_fmaf(ctx[2 * i + 1], inv, ra[2 * i + 1])));
        const unsigned off = (32 * t + r) * 128 + hh * 32;
        __builtin_amdgcn_raw_buffer_store_b128(o[0], orsrc, off, 0, 0);
        __builtin_amdgcn_raw_buffer_store_b128(o[1], orsrc, off + 16, 0, 0);
    }
}

__global__ __launch_bounds__(512, 2) void autoint_kernel(const char* __restrict__ e, const char* __restrict__ wq, const char* __restrict__ wk,
                                                         const char* __restrict__ wv, const char* __restrict__ wres, char* __restrict__ out) {
    __shared__ __attribute__((aligned(1024))) char smem[LDS_BYTES];
    const int lane = threadIdx.x & 63;
    const int wid = __builtin_amdgcn_readfirstlane(threadIdx.x >> 6);
    const int head = wid & 1;
    const int role = wid >> 2;
    const int b = blockIdx.x * 4 + (wid >> 1);
    const lds_ptr lds = (lds_ptr)smem;
    const unsigned lds0 = (unsigned)(size_t)lds;
    const int ereg = wid >> 1;

    const int w3 = wid & 3;
    const unsigned vlin = lane * 16;
    {
        unsigned long long pc; asm volatile("s_getpc_b64 %0" : "=s"(pc));
        GL1(lds0 + LDS_S + wid * SCR, pc, wid * 1024 + vlin);
    }
    __builtin_amdgcn_sched_barrier(0);
    const unsigned ve = (lane >> 3) * 128 + (((lane & 7) ^ (lane >> 4)) << 4);
    const unsigned oW = w3 * 2048 + vlin;
    const unsigned oAa = (blockIdx.x * 4 + (w3 >> 1)) * BATCH_BYTES + (w3 & 1) * 2048 + role * 4096 + ve, oAb = oAa ^ 64;
    const unsigned mWa = lds0 + LDS_W + w3 * 2048;
    const unsigned mA = lds0 + LDS_E + (w3 >> 1) * E_REGION + (w3 & 1) * 2048 + role * 4096;
    asm volatile("" :: "v"(oW), "v"(oAa), "v"(oAb), "s"(mWa), "s"(mA), "s"(mWa + 8192), "s"(mWa + 16384));
    __builtin_amdgcn_sched_barrier(0);
    asm volatile("" :: "s"(e), "s"(wq), "s"(wk), "s"(wv), "s"(wres));
    __builtin_amdgcn_sched_barrier(0);
    u32x4 q0, q1;
    if (role == 0) {
        asm volatile("global_load_dwordx4 %0, %2, %3\n\tglobal_load_dwordx4 %1, %2, %3 offset:1024" : "=&v"(q0), "=&v"(q1) : "v"(oW), "s"(wq) : "memory");
        GL2(mA, e, oAa, oAb);
        GL2(mWa + 8192, wk, oW, oW);
    } else {
        GL2(mA, e, oAa, oAb);
        GL2(mWa + 16384, wv, oW, oW);
    }
    __builtin_amdgcn_sched_barrier(0);
    char* const outp = out + (size_t)b * BATCH_BYTES + head * 64;
    unsigned lmax, lmin;
    {
        const int r = lane & 31, hh = lane >> 5;
        const unsigned ep = lds0 + LDS_E + ereg * E_REGION;
        const unsigned sp = lds0 + LDS_S + wid * SCR;
        const unsigned zp = lds0 + LDS_Z;
        const int rt = 96 + ((r & 7) ^ ((r & 16) >> 2));
        unsigned ea0 = ep + r * 128 + ((hh ^ ((r >> 1) & 7)) << 4);
        unsigned ea1 = ea0 ^ 32, ea2 = ea0 ^ 64, ea3 = ea0 ^ 96;
        unsigned et0 = ep + rt * 128 + ((hh ^ ((rt >> 1) & 7)) << 4);
        unsigned et1 = et0 ^ 32, et2 = et0 ^ 64, et3 = et0 ^ 96;
        const int i16 = lane & 15, q4 = i16 >> 2, p4 = i16 & 3, qq = lane >> 4, G1 = qq & 1;
        const int P3 = 4 * G1 + p4;
        const int wrow = (8 * hh + q4) * 64;
        unsigned wnat = lds0 + head * 4096 + wrow + P3 * 8;
        unsigned wtau = lds0 + head * 4096 + wrow + (((P3 & 1) << 2) | (P3 >> 1)) * 8;
        const bool par = (q4 & 1) == G1;
        unsigned selv = par ? 0x3f803f80u : 0u;
        unsigned cm = hh ? 0xff800000u : 0u;
        unsigned cmt = (hh && r >= 4) ? 0xff800000u : 0u;
        unsigned ktw = sp + 2048 + p4 * 64 + (q4 & 1) * 32 + (qq & 1) * 16 + (qq >> 1) * 8;
        const unsigned krd = sp + 2048 + p4 * 64 + hh * 16;
        unsigned klo = (par && i16 < 8) ? krd : zp;
        unsigned khi = (par && i16 >= 8) ? krd : zp;
        unsigned ptw = sp + 3072 + lane * 8;
        unsigned ptr = ((p4 & 1) == G1) ? sp + 3072 + (4 * hh + q4) * 8 : zp;
        unsigned sw0 = sp + r * 64 + (((2 * hh) ^ ((r >> 1) & 3)) << 4);
        unsigned sw1 = sw0 ^ 16;
        unsigned sr = sp + (lane >> 2) * 64 + (((lane & 3) ^ ((lane >> 3) & 3)) << 4);
        unsigned so = (lane >> 2) * 128 + (lane & 3) * 16;
        unsigned tso = (96 + 2 * hh) * 128 + (((r >> 2) & 1) * 16 + (r >> 3) * 4 + (r & 3)) * 2;
        const u32x4 orr = make_rsrc(outp, BATCH_BYTES - head * 64);
        const unsigned long long hmask = 0xffffffff00000000ull;
        unsigned pad0 = 0;
        u32x4 ig0 = {ea0, ea1, ea2, ea3};
        u32x4 ig1 = {et0, et1, et2, et3};
        u32x4 ig2 = {wnat, wtau, sw0, sw1};
        u32x4 ig3 = {sr, so, ktw, klo};
        u32x4 ig4 = {khi, ptw, ptr, tso};
        u32x4 ig5 = {selv, selv, selv, selv};
        u32x4 ig6 = {cm, cm, cm, cm};
        u32x4 ig7 = {cmt, pad0, pad0, pad0};
        asm volatile("" : "+v"(ig0), "+v"(ig1), "+v"(ig2), "+v"(ig3), "+v"(ig4), "+v"(ig5), "+v"(ig6), "+v"(ig7));
        __builtin_amdgcn_sched_barrier(0);
        if (role == 0) {
            asm volatile("s_waitcnt vmcnt(4)" : "+v"(q0), "+v"(q1) :: "memory");
#pragma unroll
            for (int i = 0; i < 4; ++i) {
                q0[i] = pk(__uint_as_float(q0[i] << 16) * QSCALE, __uint_as_float(q0[i] & 0xffff0000u) * QSCALE);
                q1[i] = pk(__uint_as_float(q1[i] << 16) * QSCALE, __uint_as_float(q1[i] & 0xffff0000u) * QSCALE);
            }
            *LDSP(u32x4, lds + LDS_W + oW) = q0;
            *LDSP(u32x4, lds + LDS_W + oW + 1024) = q1;
        } else {
            GL2(mA + 4096, e, oAa + 4096, oAb + 4096);
            const u32x4 ra = make_rsrc(e + (size_t)(blockIdx.x * 4 + (w3 >> 1)) * BATCH_BYTES, BATCH_BYTES);
            DMA1((w3 & 1) ? lds0 + LDS_Z : mA + 8192, ra, ve, (w3 & 1) ? 16384u : 12288u);
            const unsigned oR = (lane >> 2) * 128 + (lane & 3) * 16 + (w3 >> 1) * 64 + (w3 & 1) * 4096;
            GL2(mWa + 24576, wres, oR, oR + 1024);
            const unsigned oEa = b * BATCH_BYTES + ve, oEb = oEa ^ 64;
            const unsigned mEa = lds0 + LDS_E + ereg * E_REGION;
            const u32x4 er = make_rsrc(e + (size_t)b * BATCH_BYTES, BATCH_BYTES);
            asm volatile("s_barrier" ::: "memory");
            GL4(mEa, e, oEa, oEb);
            asm volatile("s_waitcnt vmcnt(9)\n\ts_barrier" ::: "memory");
            GL4(mEa + 4096, e, oEa + 4096, oEb + 4096);
            GL4(mEa + 8192, e, oEa + 8192, oEb + 8192);
            DMA1(mEa + 12288, er, ve, 12288u);
            asm volatile("s_waitcnt vmcnt(13)\n\ts_barrier" ::: "memory");
        }
        __builtin_amdgcn_sched_barrier(0);
        __builtin_amdgcn_s_setprio(3);
asm volatile(
    ".p2align 6\n\t"
    "s_nop 0\n\t"
    "s_nop 0\n\t"
    "s_nop 0\n\t"
    "s_movk_i32 s41, 0x1000\n\t"
    "s_movk_i32 s42, 0x2000\n\t"
    "s_cmp_lg_u32 %[role], 0\n\t"
    "s_cbranch_scc1 .Lb1_%=\n\t"
    "s_waitcnt vmcnt(0) lgkmcnt(0)\n\t"
    "s_barrier\n\t"
    "s_branch .Le1_%=\n\t"
    ".Lb1_%=:\n\t"
    "s_waitcnt vmcnt(9)\n\t"
    ".Le1_%=:\n\t"
    "ds_read_b64_tr_b16 v[252:253], v12 offset:8192\n\t"
    "ds_read_b64_tr_b16 v[254:255], v12 offset:8448\n\t"
    "ds_read_b128 v[248:251], v4 offset:0\n\t"
    "ds_read_b64_tr_b16 v[244:245], v12 offset:9216\n\t"
    "ds_read_b64_tr_b16 v[246:247], v12 offset:9472\n\t"
    "ds_read_b128 v[240:243], v5 offset:0\n\t"
    "ds_read_b64_tr_b16 v[236:237], v12 offset:10240\n\t"
    "ds_read_b64_tr_b16 v[238:239], v12 offset:10496\n\t"
    "ds_read_b128 v[232:235], v6 offset:0\n\t"
    "ds_read_b64_tr_b16 v[228:229], v12 offset:11264\n\t"
    "ds_read_b64_tr_b16 v[230:231], v12 offset:11520\n\t"
    "ds_read_b128 v[224:227], v7 offset:0\n\t"
    "ds_read_b64_tr_b16 v[220:221], v12 offset:0\n\t"
    "ds_read_b64_tr_b16 v[222:223], v12 offset:256\n\t"
    "ds_read_b64_tr_b16 v[216:217], v12 offset:1024\n\t"
    "ds_read_b64_tr_b16 v[218:219], v12 offset:1280\n\t"
    "ds_read_b64_tr_b16 v[212:213], v12 offset:2048\n\t"
    "ds_read_b64_tr_b16 v[214:215], v12 offset:2304\n\t"
    "ds_read_b64_tr_b16 v[208:209], v12 offset:3072\n\t"
    "ds_read_b64_tr_b16 v[210:211], v12 offset:3328\n\t"
    "s_waitcnt lgkmcnt(15)\n\t"
    "v_mfma_f32_32x32x16_bf16 v[40:55], v[252:255], v[248:251], 0\n\t"
    "s_waitcnt lgkmcnt(14)\n\t"
    "v_mfma_f32_32x32x16_bf16 v[40:55], v[244:247], v[240:243], v[40:55]\n\t"
    "s_waitcnt lgkmcnt(6)\n\t"
    "v_mfma_f32_32x32x16_bf16 v[56:71], v[220:223], v[248:251], 0\n\t"
    "v_mfma_f32_32x32x16_bf16 v[40:55], v[236:239], v[232:235], v[40:55]\n\t"
    "s_waitcnt lgkmcnt(0)\n\t"
    "v_mfma_f32_32x32x16_bf16 v[56:71], v[216:219], v[240:243], v[56:71]\n\t"
    "v_mfma_f32_32x32x16_bf16 v[40:55], v[228:231], v[224:227], v[40:55]\n\t"
    "v_mfma_f32_32x32x16_bf16 v[56:71], v[212:215], v[232:235], v[56:71]\n\t"
    "v_mfma_f32_32x32x16_bf16 v[56:71], v[208:211], v[224:227], v[56:71]\n\t"
    "s_nop 9\n\t"
    "v_cvt_pk_bf16_f32 v204, v40, v41\n\t"
    "v_cvt_pk_bf16_f32 v205, v42, v43\n\t"
    "v_cvt_pk_bf16_f32 v206, v44, v45\n\t"
    "v_cvt_pk_bf16_f32 v207, v46, v47\n\t"
    "v_cvt_pk_bf16_f32 v200, v48, v49\n\t"
    "v_cvt_pk_bf16_f32 v201, v50, v51\n\t"
    "v_cvt_pk_bf16_f32 v202, v52, v53\n\t"
    "v_cvt_pk_bf16_f32 v203, v54, v55\n\t"
    "v_cvt_pk_bf16_f32 v196, v56, v57\n\t"
    "v_cvt_pk_bf16_f32 v197, v58, v59\n\t"
    "v_cvt_pk_bf16_f32 v198, v60, v61\n\t"
    "v_cvt_pk_bf16_f32 v199, v62, v63\n\t"
    "v_cvt_pk_bf16_f32 v192, v64, v65\n\t"
    "v_cvt_pk_bf16_f32 v193, v66, v67\n\t"
    "v_mfma_f32_32x32x16_bf16 v[40:55], v[204:207], v[196:199], 0\n\t"
    "v_cvt_pk_bf16_f32 v194, v68, v69\n\t"
    "s_cmp_lg_u32 %[role], 0\n\t"
    "s_cbranch_scc1 .Lb2_%=\n\t"
    "s_barrier\n\t"
    "s_branch .Le2_%=\n\t"
    ".Lb2_%=:\n\t"
    "s_waitcnt vmcnt(5)\n\t"
    ".Le2_%=:\n\t"
    "ds_read_b64_tr_b16 v[188:189], v13 offset:16384\n\t"
    "ds_read_b64_tr_b16 v[190:191], v13 offset:16640\n\t"
    "ds_read_b64_tr_b16 v[184:185], v13 offset:17408\n\t"
    "ds_read_b64_tr_b16 v[186:187], v13 offset:17664\n\t"
    "ds_read_b64_tr_b16 v[180:181], v13 offset:18432\n\t"
    "ds_read_b64_tr_b16 v[182:183], v13 offset:18688\n\t"
    "ds_read_b64_tr_b16 v[176:177], v13 offset:19456\n\t"
    "ds_read_b64_tr_b16 v[178:179], v13 offset:19712\n\t"
    "ds_read_b128 v[172:175], v4 offset:4096\n\t"
    "ds_read_b128 v[168:171], v5 offset:4096\n\t"
    "ds_read_b128 v[164:167], v6 offset:4096\n\t"
    "ds_read_b128 v[160:163], v7 offset:4096\n\t"
    "v_cvt_pk_bf16_f32 v195, v70, v71\n\t"
    "s_nop 1\n\t"
    "v_mfma_f32_32x32x16_bf16 v[40:55], v[200:203], v[192:195], v[40:55]\n\t"
    "s_waitcnt lgkmcnt(10)\n\t"
    "v_mfma_f32_32x32x16_bf16 v[56:71], v[248:251], v[188:191], 0\n\t"
    "s_waitcnt lgkmcnt(8)\n\t"
    "v_mfma_f32_32x32x16_bf16 v[56:71], v[240:243], v[184:187], v[56:71]\n\t"
    "s_waitcnt lgkmcnt(5)\n\t"
    "v_mfma_f32_32x32x16_bf16 v[56:71], v[232:235], v[180:183], v[56:71]\n\t"
    "s_waitcnt lgkmcnt(2)\n\t"
    "v_mfma_f32_32x32x16_bf16 v[72:87], v[252:255], v[172:175], 0\n\t"
    "v_mfma_f32_32x32x16_bf16 v[56:71], v[224:227], v[176:179], v[56:71]\n\t"
    "v_mfma_f32_32x32x16_bf16 v[88:103], v[220:223], v[172:175], 0\n\t"
    "v_mfma_f32_32x32x16_bf16 v[104:119], v[172:175], v[188:191], 0\n\t"
    "v_mfma_f32_32x32x16_bf16 v[72:87], v[244:247], v[168:171], v[72:87]\n\t"
    "v_exp_f32_e32 v40, v40\n\t"
    "v_exp_f32_e32 v41, v41\n\t"
    "v_exp_f32_e32 v42, v42\n\t"
    "v_mfma_f32_32x32x16_bf16 v[88:103], v[216:219], v[168:171], v[88:103]\n\t"
    "v_exp_f32_e32 v43, v43\n\t"
    "v_exp_f32_e32 v44, v44\n\t"
    "v_exp_f32_e32 v45, v45\n\t"
    "v_mfma_f32_32x32x16_bf16 v[104:119], v[168:171], v[184:187], v[104:119]\n\t"
    "v_exp_f32_e32 v46, v46\n\t"
    "v_cvt_pk_bf16_f32 v248, v56, v57\n\t"
    "v_cvt_pk_bf16_f32 v249, v58, v59\n\t"
    "v_cvt_pk_bf16_f32 v250, v60, v61\n\t"
    "v_cvt_pk_bf16_f32 v251, v62, v63\n\t"
    "s_waitcnt lgkmcnt(0)\n\t"
    "v_mfma_f32_32x32x16_bf16 v[72:87], v[236:239], v[164:167], v[72:87]\n\t"
    "v_cvt_pk_bf16_f32 v240, v64, v65\n\t"
    "v_cvt_pk_bf16_f32 v241, v66, v67\n\t"
    "v_cvt_pk_bf16_f32 v242, v68, v69\n\t"
    "v_cvt_pk_bf16_f32 v243, v70, v71\n\t"
    "v_exp_f32_e32 v47, v47\n\t"
    "v_mfma_f32_32x32x16_bf16 v[88:103], v[212:215], v[164:167], v[88:103]\n\t"
    "v_exp_f32_e32 v48, v48\n\t"
    "v_exp_f32_e32 v49, v49\n\t"
    "v_exp_f32_e32 v50, v50\n\t"
    "v_mfma_f32_32x32x16_bf16 v[104:119], v[164:167], v[180:183], v[104:119]\n\t"
    "v_exp_f32_e32 v51, v51\n\t"
    "v_exp_f32_e32 v52, v52\n\t"
    "v_exp_f32_e32 v53, v53\n\t"
    "v_mfma_f32_32x32x16_bf16 v[72:87], v[228:231], v[160:163], v[72:87]\n\t"
    "v_exp_f32_e32 v54, v54\n\t"
    "v_exp_f32_e32 v55, v55\n\t"
    "v_cvt_pk_bf16_f32 v40, v40, v41\n\t"
    "v_cvt_pk_bf16_f32 v48, v48, v49\n\t"
    "v_mfma_f32_32x32x16_bf16 v[88:103], v[208:211], v[160:163], v[88:103]\n\t"
    "v_cvt_pk_bf16_f32 v41, v42, v43\n\t"
    "v_cvt_pk_bf16_f32 v49, v50, v51\n\t"
    "v_cvt_pk_bf16_f32 v42, v44, v45\n\t"
    "v_cvt_pk_bf16_f32 v43, v46, v47\n\t"
    "v_cvt_pk_bf16_f32 v50, v52, v53\n\t"
    "v_cvt_pk_bf16_f32 v51, v54, v55\n\t"
    "v_mfma_f32_16x16x32_bf16 v[232:235], v[24:27], v[40:43], 0\n\t"
    "v_cvt_pk_bf16_f32 v224, v72, v73\n\t"
    "v_cvt_pk_bf16_f32 v225, v74, v75\n\t"
    "v_mfma_f32_16x16x32_bf16 v[232:235], v[24:27], v[48:51], v[232:235]\n\t"
    "v_cvt_pk_bf16_f32 v226, v76, v77\n\t"
    "v_cvt_pk_bf16_f32 v227, v78, v79\n\t"
    "v_mfma_f32_32x32x16_bf16 v[104:119], v[160:163], v[176:179], v[104:119]\n\t"
    "v_cvt_pk_bf16_f32 v172, v80, v81\n\t"
    "v_cvt_pk_bf16_f32 v173, v82, v83\n\t"
    "v_cvt_pk_bf16_f32 v174, v84, v85\n\t"
    "v_cvt_pk_bf16_f32 v175, v86, v87\n\t"
    "v_cvt_pk_bf16_f32 v168, v88, v89\n\t"
    "v_cvt_pk_bf16_f32 v169, v90, v91\n\t"
    "v_mfma_f32_32x32x16_bf16 v[56:71], v[224:227], v[196:199], 0\n\t"
    "v_cvt_pk_bf16_f32 v170, v92, v93\n\t"
    "v_cvt_pk_bf16_f32 v171, v94, v95\n\t"
    "v_cvt_pk_bf16_f32 v164, v96, v97\n\t"
    "v_cvt_pk_bf16_f32 v165, v98, v99\n\t"
    "v_cvt_pk_bf16_f32 v166, v100, v101\n\t"
    "v_cvt_pk_bf16_f32 v167, v102, v103\n\t"
    "s_cmp_lg_u32 %[role], 0\n\t"
    "s_cbranch_scc1 .Lb3_%=\n\t"
    "s_barrier\n\t"
    "s_branch .Le3_%=\n\t"
    ".Lb3_%=:\n\t"
    "s_waitcnt vmcnt(0)\n\t"
    ".Le3_%=:\n\t"
    "ds_read_b128 v[160:163], v4 offset:8192\n\t"
    "ds_read_b128 v[156:159], v5 offset:8192\n\t"
    "ds_read_b128 v[152:155], v6 offset:8192\n\t"
    "ds_read_b128 v[148:151], v7 offset:8192\n\t"
    "v_mfma_f32_32x32x16_bf16 v[72:87], v[204:207], v[168:171], 0\n\t"
    "v_cvt_pk_bf16_f32 v144, v104, v105\n\t"
    "v_cvt_pk_bf16_f32 v145, v106, v107\n\t"
    "v_cvt_pk_bf16_f32 v146, v108, v109\n\t"
    "v_cvt_pk_bf16_f32 v147, v110, v111\n\t"
    "v_cvt_pk_bf16_f32 v140, v112, v113\n\t"
    "v_mfma_f32_32x32x16_bf16 v[56:71], v[172:175], v[192:195], v[56:71]\n\t"
    "v_mfma_f32_32x32x16_bf16 v[72:87], v[200:203], v[164:167], v[72:87]\n\t"
    "v_cvt_pk_bf16_f32 v141, v114, v115\n\t"
    "v_cvt_pk_bf16_f32 v142, v116, v117\n\t"
    "v_cvt_pk_bf16_f32 v143, v118, v119\n\t"
    "v_mfma_f32_32x32x16_bf16 v[88:103], v[248:251], v[40:43], 0\n\t"
    "v_mfma_f32_32x32x16_bf16 v[88:103], v[240:243], v[48:51], v[88:103]\n\t"
    "ds_read_b128 v[136:139], v8 offset:0\n\t"
    "ds_read_b128 v[132:135], v9 offset:0\n\t"
    "ds_read_b128 v[128:131], v10 offset:0\n\t"
    "ds_read_b128 v[124:127], v11 offset:0\n\t"
    "s_waitcnt lgkmcnt(4)\n\t"
    "v_mfma_f32_32x32x16_bf16 v[40:55], v[252:255], v[160:163], 0\n\t"
    "v_exp_f32_e32 v56, v56\n\t"
    "v_exp_f32_e32 v57, v57\n\t"
    "v_exp_f32_e32 v58, v58\n\t"
    "v_mfma_f32_32x32x16_bf16 v[40:55], v[244:247], v[156:159], v[40:55]\n\t"
    "v_exp_f32_e32 v59, v59\n\t"
    "v_exp_f32_e32 v60, v60\n\t"
    "v_exp_f32_e32 v61, v61\n\t"
    "v_mfma_f32_32x32x16_bf16 v[40:55], v[236:239], v[152:155], v[40:55]\n\t"
    "v_exp_f32_e32 v62, v62\n\t"
    "v_exp_f32_e32 v63, v63\n\t"
    "v_cvt_pk_bf16_f32 v56, v56, v57\n\t"
    "v_cvt_pk_bf16_f32 v57, v58, v59\n\t"
    "v_mfma_f32_32x32x16_bf16 v[40:55], v[228:231], v[148:151], v[40:55]\n\t"
    "v_cvt_pk_bf16_f32 v58, v60, v61\n\t"
    "v_cvt_pk_bf16_f32 v59, v62, v63\n\t"
    "v_exp_f32_e32 v64, v64\n\t"
    "v_exp_f32_e32 v65, v65\n\t"
    "v_mfma_f32_32x32x16_bf16 v[88:103], v[144:147], v[56:59], v[88:103]\n\t"
    "v_exp_f32_e32 v66, v66\n\t"
    "v_exp_f32_e32 v67, v67\n\t"
    "v_exp_f32_e32 v68, v68\n\t"
    "v_mfma_f32_16x16x32_bf16 v[232:235], v[24:27], v[56:59], v[232:235]\n\t"
    "v_exp_f32_e32 v69, v69\n\t"
    "s_waitcnt lgkmcnt(0)\n\t"
    "v_mfma_f32_16x16x32_bf16 v[120:123], v[252:255], v[136:139], 0\n\t"
    "v_exp_f32_e32 v70, v70\n\t"
    "v_mfma_f32_16x16x32_bf16 v[120:123], v[244:247], v[132:135], v[120:123]\n\t"
    "v_exp_f32_e32 v71, v71\n\t"
    "v_mfma_f32_16x16x32_bf16 v[120:123], v[236:239], v[128:131], v[120:123]\n\t"
    "v_cvt_pk_bf16_f32 v64, v64, v65\n\t"
    "v_cvt_pk_bf16_f32 v65, v66, v67\n\t"
    "v_mfma_f32_16x16x32_bf16 v[120:123], v[228:231], v[124:127], v[120:123]\n\t"
    "v_cvt_pk_bf16_f32 v66, v68, v69\n\t"
    "v_cvt_pk_bf16_f32 v67, v70, v71\n\t"
    "v_mfma_f32_32x32x16_bf16 v[104:119], v[160:163], v[188:191], 0\n\t"
    "v_exp_f32_e32 v72, v72\n\t"
    "v_exp_f32_e32 v73, v73\n\t"
    "v_exp_f32_e32 v74, v74\n\t"
    "v_mfma_f32_32x32x16_bf16 v[88:103], v[140:143], v[64:67], v[88:103]\n\t"
    "v_exp_f32_e32 v75, v75\n\t"
    "v_exp_f32_e32 v76, v76\n\t"
    "v_exp_f32_e32 v77, v77\n\t"
    "v_mfma_f32_32x32x16_bf16 v[104:119], v[156:159], v[184:187], v[104:119]\n\t"
    "v_exp_f32_e32 v78, v78\n\t"
    "v_exp_f32_e32 v79, v79\n\t"
    "v_exp_f32_e32 v80, v80\n\t"
    "v_mfma_f32_32x32x16_bf16 v[104:119], v[152:155], v[180:183], v[104:119]\n\t"
    "v_exp_f32_e32 v81, v81\n\t"
    "v_exp_f32_e32 v82, v82\n\t"
    "v_exp_f32_e32 v83, v83\n\t"
    "v_mfma_f32_32x32x16_bf16 v[104:119], v[148:151], v[176:179], v[104:119]\n\t"
    "v_exp_f32_e32 v84, v84\n\t"
    "v_exp_f32_e32 v85, v85\n\t"
    "v_exp_f32_e32 v86, v86\n\t"
    "v_mfma_f32_16x16x32_bf16 v[252:255], v[136:139], v[188:191], 0\n\t"
    "v_exp_f32_e32 v87, v87\n\t"
    "v_mfma_f32_16x16x32_bf16 v[252:255], v[132:135], v[184:187], v[252:255]\n\t"
    "v_cvt_pk_bf16_f32 v244, v40, v41\n\t"
    "v_cvt_pk_bf16_f32 v245, v42, v43\n\t"
    "v_mfma_f32_16x16x32_bf16 v[252:255], v[128:131], v[180:183], v[252:255]\n\t"
    "v_cvt_pk_bf16_f32 v246, v44, v45\n\t"
    "v_cvt_pk_bf16_f32 v247, v46, v47\n\t"
    "v_mfma_f32_16x16x32_bf16 v[252:255], v[124:127], v[176:179], v[252:255]\n\t"
    "v_cvt_pk_bf16_f32 v236, v104, v105\n\t"
    "v_cvt_pk_bf16_f32 v237, v106, v107\n\t"
    "v_mfma_f32_32x32x16_bf16 v[176:191], v[244:247], v[196:199], 0\n\t"
    "v_cvt_pk_bf16_f32 v238, v108, v109\n\t"
    "v_cvt_pk_bf16_f32 v239, v110, v111\n\t"
    "v_cvt_pk_bf16_f32 v228, v112, v113\n\t"
    "v_cvt_pk_bf16_f32 v229, v114, v115\n\t"
    "v_cvt_pk_bf16_f32 v230, v116, v117\n\t"
    "v_cvt_pk_bf16_f32 v231, v118, v119\n\t"
    "v_mfma_f32_32x32x16_bf16 v[104:119], v[224:227], v[168:171], 0\n\t"
    "v_cvt_pk_bf16_f32 v8, v48, v49\n\t"
    "v_cvt_pk_bf16_f32 v9, v50, v51\n\t"
    "v_cvt_pk_bf16_f32 v10, v52, v53\n\t"
    "v_cvt_pk_bf16_f32 v11, v54, v55\n\t"
    "v_cvt_pk_bf16_f32 v72, v72, v73\n\t"
    "v_cvt_pk_bf16_f32 v73, v74, v75\n\t"
    "v_mfma_f32_32x32x16_bf16 v[104:119], v[172:175], v[164:167], v[104:119]\n\t"
    "v_cvt_pk_bf16_f32 v74, v76, v77\n\t"
    "v_cvt_pk_bf16_f32 v75, v78, v79\n\t"
    "v_cvt_pk_bf16_f32 v80, v80, v81\n\t"
    "v_cvt_pk_bf16_f32 v81, v82, v83\n\t"
    "v_cvt_pk_bf16_f32 v82, v84, v85\n\t"
    "v_cvt_pk_bf16_f32 v83, v86, v87\n\t"
    "v_mfma_f32_32x32x16_bf16 v[176:191], v[8:11], v[192:195], v[176:191]\n\t"
    "v_cvt_pk_bf16_f32 v120, v120, v121\n\t"
    "v_cvt_pk_bf16_f32 v121, v122, v123\n\t"
    "ds_write_b64 v18, v[120:121] offset:0\n\t"
    "v_mfma_f32_32x32x16_bf16 v[40:55], v[220:223], v[160:163], 0\n\t"
    "ds_read_b128 v[160:163], v19 offset:0\n\t"
    "ds_read_b128 v[120:123], v19 offset:32\n\t"
    "v_exp_f32_e32 v104, v104\n\t"
    "v_exp_f32_e32 v105, v105\n\t"
    "v_exp_f32_e32 v106, v106\n\t"
    "v_mfma_f32_32x32x16_bf16 v[40:55], v[216:219], v[156:159], v[40:55]\n\t"
    "v_exp_f32_e32 v107, v107\n\t"
    "v_exp_f32_e32 v108, v108\n\t"
    "v_exp_f32_e32 v109, v109\n\t"
    "v_mfma_f32_32x32x16_bf16 v[40:55], v[212:215], v[152:155], v[40:55]\n\t"
    "v_exp_f32_e32 v110, v110\n\t"
    "v_exp_f32_e32 v111, v111\n\t"
    "v_exp_f32_e32 v112, v112\n\t"
    "v_mfma_f32_32x32x16_bf16 v[40:55], v[208:211], v[148:151], v[40:55]\n\t"
    "v_exp_f32_e32 v113, v113\n\t"
    "v_exp_f32_e32 v114, v114\n\t"
    "v_exp_f32_e32 v115, v115\n\t"
    "v_mfma_f32_16x16x32_bf16 v[156:159], v[24:27], v[72:75], 0\n\t"
    "v_exp_f32_e32 v116, v116\n\t"
    "v_mfma_f32_16x16x32_bf16 v[156:159], v[24:27], v[80:83], v[156:159]\n\t"
    "v_exp_f32_e32 v117, v117\n\t"
    "s_waitcnt lgkmcnt(0)\n\t"
    "v_mfma_f32_16x16x32_bf16 v[152:155], v[160:163], v[196:199], v[28:31]\n\t"
    "v_exp_f32_e32 v118, v118\n\t"
    "v_mfma_f32_16x16x32_bf16 v[152:155], v[120:123], v[192:195], v[152:155]\n\t"
    "v_exp_f32_e32 v119, v119\n\t"
    "v_mfma_f32_16x16x32_bf16 v[196:199], v[220:223], v[136:139], 0\n\t"
    "v_cvt_pk_bf16_f32 v104, v104, v105\n\t"
    "v_cvt_pk_bf16_f32 v112, v112, v113\n\t"
    "v_mfma_f32_16x16x32_bf16 v[196:199], v[216:219], v[132:135], v[196:199]\n\t"
    "v_cvt_pk_bf16_f32 v105, v106, v107\n\t"
    "v_cvt_pk_bf16_f32 v113, v114, v115\n\t"
    "v_mfma_f32_16x16x32_bf16 v[196:199], v[212:215], v[128:131], v[196:199]\n\t"
    "v_cvt_pk_bf16_f32 v106, v108, v109\n\t"
    "v_cvt_pk_bf16_f32 v107, v110, v111\n\t"
    "v_mfma_f32_16x16x32_bf16 v[196:199], v[208:211], v[124:127], v[196:199]\n\t"
    "v_cvt_pk_bf16_f32 v114, v116, v117\n\t"
    "v_cvt_pk_bf16_f32 v115, v118, v119\n\t"
    "v_mfma_f32_32x32x16_bf16 v[208:223], v[248:251], v[72:75], 0\n\t"
    "v_exp_f32_e32 v176, v176\n\t"
    "v_exp_f32_e32 v177, v177\n\t"
    "v_exp_f32_e32 v178, v178\n\t"
    "v_mfma_f32_32x32x16_bf16 v[208:223], v[240:243], v[80:83], v[208:223]\n\t"
    "v_exp_f32_e32 v179, v179\n\t"
    "v_exp_f32_e32 v180, v180\n\t"
    "v_exp_f32_e32 v181, v181\n\t"
    "v_mfma_f32_16x16x32_bf16 v[156:159], v[24:27], v[104:107], v[156:159]\n\t"
    "v_exp_f32_e32 v182, v182\n\t"
    "v_exp_f32_e32 v183, v183\n\t"
    "v_exp_f32_e32 v184, v184\n\t"
    "v_exp_f32_e32 v185, v185\n\t"
    "v_exp_f32_e32 v186, v186\n\t"
    "v_exp_f32_e32 v187, v187\n\t"
    "v_exp_f32_e32 v188, v188\n\t"
    "v_exp_f32_e32 v189, v189\n\t"
    "v_exp_f32_e32 v190, v190\n\t"
    "v_exp_f32_e32 v191, v191\n\t"
    "ds_read_b64_tr_b16 v[192:193], v13 offset:24576\n\t"
    "ds_read_b64_tr_b16 v[194:195], v13 offset:24832\n\t"
    "ds_read_b64_tr_b16 v[148:149], v13 offset:25600\n\t"
    "ds_read_b64_tr_b16 v[150:151], v13 offset:25856\n\t"
    "ds_read_b64_tr_b16 v[84:85], v13 offset:26624\n\t"
    "ds_read_b64_tr_b16 v[86:87], v13 offset:26880\n\t"
    "ds_read_b64_tr_b16 v[80:81], v13 offset:27648\n\t"
    "ds_read_b64_tr_b16 v[82:83], v13 offset:27904\n\t"
    "v_exp_f32_e32 v152, v152\n\t"
    "v_exp_f32_e32 v153, v153\n\t"
    "v_exp_f32_e32 v154, v154\n\t"
    "v_cvt_pk_bf16_f32 v76, v152, v153\n\t"
    "v_exp_f32_e32 v155, v155\n\t"
    "v_cvt_pk_bf16_f32 v176, v176, v177\n\t"
    "v_cvt_pk_bf16_f32 v77, v154, v155\n\t"
    "v_mfma_f32_16x16x32_bf16 v[232:235], v[24:27], v[64:67], v[232:235]\n\t"
    "v_cvt_pk_bf16_f32 v177, v178, v179\n\t"
    "v_cvt_pk_bf16_f32 v178, v180, v181\n\t"
    "v_mfma_f32_32x32x16_bf16 v[56:71], v[244:247], v[168:171], 0\n\t"
    "v_cvt_pk_bf16_f32 v179, v182, v183\n\t"
    "v_cvt_pk_bf16_f32 v184, v184, v185\n\t"
    "v_cvt_pk_bf16_f32 v185, v186, v187\n\t"
    "v_cvt_pk_bf16_f32 v186, v188, v189\n\t"
    "v_cvt_pk_bf16_f32 v187, v190, v191\n\t"
    "v_cvt_pk_bf16_f32 v252, v252, v253\n\t"
    "v_mfma_f32_32x32x16_bf16 v[88:103], v[236:239], v[176:179], v[88:103]\n\t"
    "ds_read_b128 v[152:155], v4 offset:0\n\t"
    "ds_read_b128 v[72:75], v5 offset:0\n\t"
    "v_cvt_pk_bf16_f32 v253, v254, v255\n\t"
    "v_mov_b32_e32 v254, 0\n\t"
    "v_mov_b32_e32 v255, 0\n\t"
    "v_cvt_pk_bf16_f32 v196, v196, v197\n\t"
    "v_cvt_pk_bf16_f32 v197, v198, v199\n\t"
    "ds_write_b64 v18, v[196:197] offset:256\n\t"
    "v_mov_b32_e32 v78, 0\n\t"
    "v_mfma_f32_32x32x16_bf16 v[88:103], v[228:231], v[184:187], v[88:103]\n\t"
    "ds_read_b128 v[196:199], v19 offset:256\n\t"
    "v_mov_b32_e32 v79, 0\n\t"
    "v_mfma_f32_32x32x16_bf16 v[56:71], v[8:11], v[164:167], v[56:71]\n\t"
    "v_mfma_f32_16x16x32_bf16 v[232:235], v[24:27], v[176:179], v[232:235]\n\t"
    "v_mfma_f32_32x32x16_bf16 v[88:103], v[252:255], v[76:79], v[88:103]\n\t"
    "v_mfma_f32_16x16x32_bf16 v[232:235], v[24:27], v[184:187], v[232:235]\n\t"
    "ds_read_b128 v[188:191], v19 offset:288\n\t"
    "ds_read_b128 v[184:187], v6 offset:0\n\t"
    "ds_read_b128 v[180:183], v7 offset:0\n\t"
    "s_waitcnt lgkmcnt(4)\n\t"
    "v_mfma_f32_16x16x32_bf16 v[176:179], v[136:139], v[192:195], 0\n\t"
    "v_mfma_f32_16x16x32_bf16 v[232:235], v[24:27], v[76:79], v[232:235]\n\t"
    "v_mfma_f32_16x16x32_bf16 v[176:179], v[132:135], v[148:151], v[176:179]\n\t"
    "v_mfma_f32_16x16x32_bf16 v[156:159], v[24:27], v[112:115], v[156:159]\n\t"
    "v_mfma_f32_16x16x32_bf16 v[176:179], v[128:131], v[84:87], v[176:179]\n\t"
    "v_exp_f32_e32 v56, v56\n\t"
    "v_mfma_f32_16x16x32_bf16 v[176:179], v[124:127], v[80:83], v[176:179]\n\t"
    "v_exp_f32_e32 v57, v57\n\t"
    "v_mfma_f32_32x32x16_bf16 v[124:139], v[192:195], v[152:155], 0\n\t"
    "v_cvt_pk_bf16_f32 v56, v56, v57\n\t"
    "v_rcp_f32_e32 v155, v232\n\t"
    "v_exp_f32_e32 v58, v58\n\t"
    "v_exp_f32_e32 v59, v59\n\t"
    "v_mfma_f32_32x32x16_bf16 v[124:139], v[148:151], v[72:75], v[124:139]\n\t"
    "v_cvt_pk_bf16_f32 v57, v58, v59\n\t"
    "v_exp_f32_e32 v60, v60\n\t"
    "v_exp_f32_e32 v61, v61\n\t"
    "v_exp_f32_e32 v62, v62\n\t"
    "s_waitcnt lgkmcnt(0)\n\t"
    "v_mfma_f32_32x32x16_bf16 v[124:139], v[84:87], v[184:187], v[124:139]\n\t"
    "v_cvt_pk_bf16_f32 v58, v60, v61\n\t"
    "v_exp_f32_e32 v63, v63\n\t"
    "v_exp_f32_e32 v64, v64\n\t"
    "v_cvt_pk_bf16_f32 v59, v62, v63\n\t"
    "v_mfma_f32_32x32x16_bf16 v[124:139], v[80:83], v[180:183], v[124:139]\n\t"
    "v_exp_f32_e32 v65, v65\n\t"
    "v_exp_f32_e32 v66, v66\n\t"
    "v_cvt_pk_bf16_f32 v64, v64, v65\n\t"
    "v_exp_f32_e32 v67, v67\n\t"
    "v_mfma_f32_16x16x32_bf16 v[156:159], v[24:27], v[56:59], v[156:159]\n\t"
    "v_cvt_pk_bf16_f32 v65, v66, v67\n\t"
    "v_exp_f32_e32 v68, v68\n\t"
    "v_mfma_f32_16x16x32_bf16 v[184:187], v[160:163], v[168:171], v[28:31]\n\t"
    "v_exp_f32_e32 v69, v69\n\t"
    "v_mfma_f32_16x16x32_bf16 v[184:187], v[120:123], v[164:167], v[184:187]\n\t"
    "v_cvt_pk_bf16_f32 v66, v68, v69\n\t"
    "v_exp_f32_e32 v70, v70\n\t"
    "v_mfma_f32_32x32x16_bf16 v[208:223], v[144:147], v[104:107], v[208:223]\n\t"
    "v_exp_f32_e32 v71, v71\n\t"
    "v_fmac_f32_e32 v124, v88, v155\n\t"
    "v_cvt_pk_bf16_f32 v67, v70, v71\n\t"
    "v_fmac_f32_e32 v125, v89, v155\n\t"
    "v_cvt_pk_bf16_f32 v124, v124, v125\n\t"
    "v_mfma_f32_16x16x32_bf16 v[156:159], v[24:27], v[64:67], v[156:159]\n\t"
    "v_exp_f32_e32 v184, v184\n\t"
    "v_mfma_f32_32x32x16_bf16 v[208:223], v[140:143], v[112:115], v[208:223]\n\t"
    "v_exp_f32_e32 v185, v185\n\t"
    "v_exp_f32_e32 v186, v186\n\t"
    "v_exp_f32_e32 v187, v187\n\t"
    "v_mfma_f32_32x32x16_bf16 v[208:223], v[236:239], v[56:59], v[208:223]\n\t"
    "v_cvt_pk_bf16_f32 v76, v184, v185\n\t"
    "v_cvt_pk_bf16_f32 v77, v186, v187\n\t"
    "ds_read_b128 v[184:187], v4 offset:4096\n\t"
    "ds_read_b128 v[180:183], v5 offset:4096\n\t"
    "ds_read_b128 v[168:171], v6 offset:4096\n\t"
    "ds_read_b128 v[164:167], v7 offset:4096\n\t"
    "v_pk_max_i16 v124, v124, 0\n\t"
    "v_fmac_f32_e32 v126, v90, v155\n\t"
    "v_fmac_f32_e32 v127, v91, v155\n\t"
    "v_cvt_pk_bf16_f32 v125, v126, v127\n\t"
    "v_mfma_f32_16x16x32_bf16 v[156:159], v[24:27], v[76:79], v[156:159]\n\t"
    "v_pk_max_i16 v125, v125, 0\n\t"
    "v_fmac_f32_e32 v128, v92, v155\n\t"
    "v_mfma_f32_32x32x16_bf16 v[208:223], v[228:231], v[64:67], v[208:223]\n\t"
    "v_fmac_f32_e32 v129, v93, v155\n\t"
    "v_cvt_pk_bf16_f32 v126, v128, v129\n\t"
    "v_pk_max_i16 v126, v126, 0\n\t"
    "v_fmac_f32_e32 v130, v94, v155\n\t"
    "v_fmac_f32_e32 v131, v95, v155\n\t"
    "v_cvt_pk_bf16_f32 v127, v130, v131\n\t"
    "v_pk_max_i16 v127, v127, 0\n\t"
    "ds_write_b128 v14, v[124:127] offset:0\n\t"
    "ds_read_b128 v[116:119], v20 offset:256\n\t"
    "v_cvt_pk_bf16_f32 v112, v40, v41\n\t"
    "ds_read_b128 v[108:111], v20 offset:288\n\t"
    "v_cvt_pk_bf16_f32 v113, v42, v43\n\t"
    "v_cvt_pk_bf16_f32 v114, v44, v45\n\t"
    "v_cvt_pk_bf16_f32 v115, v46, v47\n\t"
    "v_cvt_pk_bf16_f32 v104, v48, v49\n\t"
    "v_rcp_f32_e32 v154, v156\n\t"
    "v_mfma_f32_32x32x16_bf16 v[56:71], v[204:207], v[112:115], 0\n\t"
    "v_cvt_pk_bf16_f32 v105, v50, v51\n\t"
    "v_cvt_pk_bf16_f32 v106, v52, v53\n\t"
    "v_fmac_f32_e32 v132, v96, v155\n\t"
    "v_cvt_pk_bf16_f32 v107, v54, v55\n\t"
    "v_mov_b32_e32 v36, v232\n\t"
    "v_fmac_f32_e32 v133, v97, v155\n\t"
    "v_mfma_f32_32x32x16_bf16 v[40:55], v[244:247], v[112:115], 0\n\t"
    "v_cvt_pk_bf16_f32 v132, v132, v133\n\t"
    "v_pk_max_i16 v132, v132, 0\n\t"
    "v_mov_b32_e32 v37, v232\n\t"
    "v_fmac_f32_e32 v134, v98, v155\n\t"
    "v_fmac_f32_e32 v135, v99, v155\n\t"
    "v_cvt_pk_bf16_f32 v133, v134, v135\n\t"
    "v_mfma_f32_32x32x16_bf16 v[56:71], v[200:203], v[104:107], v[56:71]\n\t"
    "v_pk_max_i16 v133, v133, 0\n\t"
    "v_fmac_f32_e32 v136, v100, v155\n\t"
    "v_fmac_f32_e32 v137, v101, v155\n\t"
    "v_cvt_pk_bf16_f32 v134, v136, v137\n\t"
    "v_pk_max_i16 v134, v134, 0\n\t"
    "v_fmac_f32_e32 v138, v102, v155\n\t"
    "v_mfma_f32_32x32x16_bf16 v[40:55], v[8:11], v[104:107], v[40:55]\n\t"
    "v_fmac_f32_e32 v139, v103, v155\n\t"
    "v_cvt_pk_bf16_f32 v135, v138, v139\n\t"
    "v_pk_max_i16 v135, v135, 0\n\t"
    "ds_write_b128 v15, v[132:135] offset:0\n\t"
    "s_waitcnt lgkmcnt(2)\n\t"
    "v_mfma_f32_32x32x16_bf16 v[88:103], v[192:195], v[184:187], 0\n\t"
    "v_exp_f32_e32 v56, v56\n\t"
    "v_exp_f32_e32 v57, v57\n\t"
    "v_exp_f32_e32 v58, v58\n\t"
    "v_mfma_f32_32x32x16_bf16 v[88:103], v[148:151], v[180:183], v[88:103]\n\t"
    "v_exp_f32_e32 v59, v59\n\t"
    "v_exp_f32_e32 v60, v60\n\t"
    "v_exp_f32_e32 v61, v61\n\t"
    "v_mfma_f32_32x32x16_bf16 v[88:103], v[84:87], v[168:171], v[88:103]\n\t"
    "v_exp_f32_e32 v62, v62\n\t"
    "v_exp_f32_e32 v63, v63\n\t"
    "ds_read_b128 v[232:235], v16 offset:0\n\t"
    "ds_read_b128 v[184:187], v16 offset:1024\n\t"
    "v_exp_f32_e32 v64, v64\n\t"
    "v_mfma_f32_32x32x16_bf16 v[88:103], v[80:83], v[164:167], v[88:103]\n\t"
    "v_exp_f32_e32 v65, v65\n\t"
    "v_exp_f32_e32 v66, v66\n\t"
    "v_exp_f32_e32 v67, v67\n\t"
    "v_exp_f32_e32 v68, v68\n\t"
    "v_exp_f32_e32 v69, v69\n\t"
    "v_exp_f32_e32 v70, v70\n\t"
    "v_exp_f32_e32 v71, v71\n\t"
    "ds_read_b128 v[180:183], v4 offset:8192\n\t"
    "ds_read_b128 v[168:171], v5 offset:8192\n\t"
    "ds_read_b128 v[164:167], v6 offset:8192\n\t"
    "ds_read_b128 v[136:139], v7 offset:8192\n\t"
    "v_mfma_f32_32x32x16_bf16 v[208:223], v[252:255], v[76:79], v[208:223]\n\t"
    "v_max_u32_e32 v36, v156, v36\n\t"
    "v_min_u32_e32 v37, v156, v37\n\t"
    "v_mov_b32_e32 v156, v32\n\t"
    "v_mov_b32_e32 v157, v32\n\t"
    "v_mov_b32_e32 v158, v32\n\t"
    "v_mov_b32_e32 v159, v32\n\t"
    "v_mfma_f32_16x16x32_bf16 v[132:135], v[196:199], v[204:207], 0\n\t"
    "s_waitcnt lgkmcnt(5)\n\t"
    "buffer_store_dwordx4 v[232:235], v17, %[orsrc], 0 offen offset:0 sc1\n\t"
    "s_waitcnt lgkmcnt(4)\n\t"
    "buffer_store_dwordx4 v[184:187], v17, %[orsrc], 0 offen offset:2048 sc1\n\t"
    "v_mov_b32_e32 v232, 0x3f803f80\n\t"
    "v_fmac_f32_e32 v88, v208, v154\n\t"
    "v_mfma_f32_16x16x32_bf16 v[132:135], v[188:191], v[200:203], v[132:135]\n\t"
    "v_fmac_f32_e32 v89, v209, v154\n\t"
    "v_fmac_f32_e32 v90, v210, v154\n\t"
    "v_mfma_f32_16x16x32_bf16 v[132:135], v[116:119], v[224:227], v[132:135]\n\t"
    "v_fmac_f32_e32 v91, v211, v154\n\t"
    "v_fmac_f32_e32 v92, v212, v154\n\t"
    "v_mfma_f32_16x16x32_bf16 v[132:135], v[108:111], v[172:175], v[132:135]\n\t"
    "v_fmac_f32_e32 v93, v213, v154\n\t"
    "v_fmac_f32_e32 v94, v214, v154\n\t"
    "v_mfma_f32_16x16x32_bf16 v[204:207], v[196:199], v[244:247], v[156:159]\n\t"
    "v_fmac_f32_e32 v95, v215, v154\n\t"
    "v_fmac_f32_e32 v96, v216, v154\n\t"
    "v_mfma_f32_16x16x32_bf16 v[204:207], v[188:191], v[8:11], v[204:207]\n\t"
    "v_fmac_f32_e32 v97, v217, v154\n\t"
    "v_fmac_f32_e32 v98, v218, v154\n\t"
    "v_mfma_f32_16x16x32_bf16 v[204:207], v[116:119], v[160:163], v[204:207]\n\t"
    "v_fmac_f32_e32 v99, v219, v154\n\t"
    "v_fmac_f32_e32 v100, v220, v154\n\t"
    "v_mfma_f32_16x16x32_bf16 v[204:207], v[108:111], v[120:123], v[204:207]\n\t"
    "v_fmac_f32_e32 v101, v221, v154\n\t"
    "v_fmac_f32_e32 v102, v222, v154\n\t"
    "s_setprio 2\n\t"
    "v_fmac_f32_e32 v103, v223, v154\n\t"
    "v_mfma_f32_32x32x16_bf16 v[208:223], v[224:227], v[112:115], 0\n\t"
    "v_cvt_pk_bf16_f32 v88, v88, v89\n\t"
    "v_cvt_pk_bf16_f32 v96, v96, v97\n\t"
    "v_cvt_pk_bf16_f32 v89, v90, v91\n\t"
    "v_pk_max_i16 v88, v88, 0\n\t"
    "v_cvt_pk_bf16_f32 v97, v98, v99\n\t"
    "v_pk_max_i16 v96, v96, 0\n\t"
    "v_mfma_f32_32x32x16_bf16 v[208:223], v[172:175], v[104:107], v[208:223]\n\t"
    "v_cvt_pk_bf16_f32 v90, v92, v93\n\t"
    "v_cvt_pk_bf16_f32 v91, v94, v95\n\t"
    "v_pk_max_i16 v89, v89, 0\n\t"
    "v_cvt_pk_bf16_f32 v98, v100, v101\n\t"
    "v_cvt_pk_bf16_f32 v99, v102, v103\n\t"
    "v_pk_max_i16 v97, v97, 0\n\t"
    "v_pk_max_i16 v90, v90, 0\n\t"
    "v_pk_max_i16 v91, v91, 0\n\t"
    "ds_write_b128 v14, v[88:91] offset:0\n\t"
    "v_pk_max_i16 v98, v98, 0\n\t"
    "v_pk_max_i16 v99, v99, 0\n\t"
    "ds_write_b128 v15, v[96:99] offset:0\n\t"
    "ds_read_b128 v[244:247], v16 offset:0\n\t"
    "ds_read_b128 v[224:227], v16 offset:1024\n\t"
    "v_exp_f32_e32 v132, v132\n\t"
    "v_exp_f32_e32 v133, v133\n\t"
    "v_exp_f32_e32 v134, v134\n\t"
    "v_exp_f32_e32 v135, v135\n\t"
    "v_cvt_pk_bf16_f32 v132, v132, v133\n\t"
    "v_cvt_pk_bf16_f32 v133, v134, v135\n\t"
    "ds_write_b64 v21, v[132:133] offset:0\n\t"
    "v_exp_f32_e32 v204, v204\n\t"
    "v_exp_f32_e32 v205, v205\n\t"
    "v_exp_f32_e32 v206, v206\n\t"
    "v_exp_f32_e32 v207, v207\n\t"
    "v_cvt_pk_bf16_f32 v204, v204, v205\n\t"
    "v_cvt_pk_bf16_f32 v205, v206, v207\n\t"
    "ds_write_b64 v21, v[204:205] offset:512\n\t"
    "ds_read_b64_tr_b16 v[206:207], v22 offset:64\n\t"
    "ds_read_b64_tr_b16 v[200:201], v22 offset:128\n\t"
    "ds_read_b64_tr_b16 v[204:205], v22 offset:0\n\t"
    "ds_read_b64_tr_b16 v[202:203], v22 offset:192\n\t"
    "ds_read_b64_tr_b16 v[196:197], v22 offset:256\n\t"
    "ds_read_b64_tr_b16 v[198:199], v22 offset:320\n\t"
    "ds_read_b64_tr_b16 v[188:189], v22 offset:384\n\t"
    "ds_read_b64_tr_b16 v[190:191], v22 offset:448\n\t"
    "ds_read_b64_tr_b16 v[184:185], v22 offset:512\n\t"
    "s_waitcnt lgkmcnt(11)\n\t"
    "buffer_store_dwordx4 v[244:247], v17, %[orsrc], s41 offen offset:0 sc1\n\t"
    "buffer_store_dwordx4 v[224:227], v17, %[orsrc], s41 offen offset:2048 sc1\n\t"
    "ds_read_b64_tr_b16 v[186:187], v22 offset:576\n\t"
    "ds_read_b64_tr_b16 v[244:245], v22 offset:640\n\t"
    "ds_read_b64_tr_b16 v[246:247], v22 offset:704\n\t"
    "ds_read_b64_tr_b16 v[224:225], v22 offset:768\n\t"
    "ds_read_b64_tr_b16 v[226:227], v22 offset:832\n\t"
    "v_mfma_f32_32x32x16_bf16 v[88:103], v[192:195], v[180:183], 0\n\t"
    "v_exp_f32_e32 v208, v208\n\t"
    "v_exp_f32_e32 v209, v209\n\t"
    "v_exp_f32_e32 v210, v210\n\t"
    "v_mfma_f32_32x32x16_bf16 v[88:103], v[148:151], v[168:171], v[88:103]\n\t"
    "v_exp_f32_e32 v211, v211\n\t"
    "v_exp_f32_e32 v212, v212\n\t"
    "v_exp_f32_e32 v213, v213\n\t"
    "v_mfma_f32_32x32x16_bf16 v[88:103], v[84:87], v[164:167], v[88:103]\n\t"
    "v_exp_f32_e32 v214, v214\n\t"
    "v_exp_f32_e32 v215, v215\n\t"
    "v_exp_f32_e32 v216, v216\n\t"
    "v_mfma_f32_32x32x16_bf16 v[88:103], v[80:83], v[136:139], v[88:103]\n\t"
    "v_exp_f32_e32 v217, v217\n\t"
    "v_exp_f32_e32 v218, v218\n\t"
    "v_exp_f32_e32 v219, v219\n\t"
    "v_exp_f32_e32 v220, v220\n\t"
    "v_exp_f32_e32 v221, v221\n\t"
    "v_exp_f32_e32 v222, v222\n\t"
    "v_exp_f32_e32 v223, v223\n\t"
    "v_cvt_pk_bf16_f32 v208, v208, v209\n\t"
    "v_cvt_pk_bf16_f32 v216, v216, v217\n\t"
    "v_cvt_pk_bf16_f32 v209, v210, v211\n\t"
    "v_cvt_pk_bf16_f32 v217, v218, v219\n\t"
    "v_cvt_pk_bf16_f32 v210, v212, v213\n\t"
    "v_cvt_pk_bf16_f32 v211, v214, v215\n\t"
    "v_cvt_pk_bf16_f32 v218, v220, v221\n\t"
    "s_setprio 1\n\t"
    "v_cvt_pk_bf16_f32 v219, v222, v223\n\t"
    "v_mfma_f32_16x16x32_bf16 v[192:195], v[160:163], v[112:115], v[28:31]\n\t"
    "v_mov_b32_e32 v233, 0x3f803f80\n\t"
    "v_mov_b32_e32 v234, 0x3f803f80\n\t"
    "v_mfma_f32_16x16x32_bf16 v[192:195], v[120:123], v[104:107], v[192:195]\n\t"
    "v_mov_b32_e32 v235, 0x3f803f80\n\t"
    "v_cvt_pk_bf16_f32 v56, v56, v57\n\t"
    "s_waitcnt lgkmcnt(0)\n\t"
    "v_mfma_f32_16x16x32_bf16 v[180:183], v[204:207], v[232:235], 0\n\t"
    "v_cvt_pk_bf16_f32 v57, v58, v59\n\t"
    "v_cvt_pk_bf16_f32 v58, v60, v61\n\t"
    "v_mfma_f32_16x16x32_bf16 v[180:183], v[200:203], v[232:235], v[180:183]\n\t"
    "v_cvt_pk_bf16_f32 v59, v62, v63\n\t"
    "v_cvt_pk_bf16_f32 v64, v64, v65\n\t"
    "v_mfma_f32_16x16x32_bf16 v[180:183], v[196:199], v[232:235], v[180:183]\n\t"
    "v_cvt_pk_bf16_f32 v65, v66, v67\n\t"
    "v_cvt_pk_bf16_f32 v66, v68, v69\n\t"
    "v_mfma_f32_32x32x16_bf16 v[104:119], v[248:251], v[56:59], 0\n\t"
    "v_cvt_pk_bf16_f32 v67, v70, v71\n\t"
    "v_exp_f32_e32 v40, v40\n\t"
    "v_exp_f32_e32 v41, v41\n\t"
    "v_exp_f32_e32 v42, v42\n\t"
    "v_mfma_f32_16x16x32_bf16 v[172:175], v[24:27], v[56:59], 0\n\t"
    "v_exp_f32_e32 v43, v43\n\t"
    "v_mfma_f32_32x32x16_bf16 v[104:119], v[240:243], v[64:67], v[104:119]\n\t"
    "v_exp_f32_e32 v44, v44\n\t"
    "v_exp_f32_e32 v45, v45\n\t"
    "v_exp_f32_e32 v46, v46\n\t"
    "v_mfma_f32_16x16x32_bf16 v[172:175], v[24:27], v[64:67], v[172:175]\n\t"
    "v_exp_f32_e32 v47, v47\n\t"
    "v_mfma_f32_32x32x16_bf16 v[104:119], v[144:147], v[208:211], v[104:119]\n\t"
    "v_cvt_pk_bf16_f32 v40, v40, v41\n\t"
    "v_cvt_pk_bf16_f32 v41, v42, v43\n\t"
    "v_cvt_pk_bf16_f32 v42, v44, v45\n\t"
    "v_cvt_pk_bf16_f32 v43, v46, v47\n\t"
    "v_exp_f32_e32 v48, v48\n\t"
    "v_mfma_f32_16x16x32_bf16 v[172:175], v[24:27], v[208:211], v[172:175]\n\t"
    "v_exp_f32_e32 v49, v49\n\t"
    "v_mfma_f32_16x16x32_bf16 v[172:175], v[24:27], v[216:219], v[172:175]\n\t"
    "v_exp_f32_e32 v50, v50\n\t"
    "v_mfma_f32_16x16x32_bf16 v[172:175], v[24:27], v[40:43], v[172:175]\n\t"
    "v_exp_f32_e32 v51, v51\n\t"
    "v_mfma_f32_16x16x32_bf16 v[180:183], v[188:191], v[232:235], v[180:183]\n\t"
    "v_exp_f32_e32 v52, v52\n\t"
    "v_mfma_f32_16x16x32_bf16 v[180:183], v[184:187], v[232:235], v[180:183]\n\t"
    "v_exp_f32_e32 v53, v53\n\t"
    "v_mfma_f32_16x16x32_bf16 v[180:183], v[244:247], v[232:235], v[180:183]\n\t"
    "v_exp_f32_e32 v54, v54\n\t"
    "v_mfma_f32_16x16x32_bf16 v[180:183], v[224:227], v[232:235], v[180:183]\n\t"
    "v_exp_f32_e32 v55, v55\n\t"
    "v_mfma_f32_32x32x16_bf16 v[104:119], v[140:143], v[216:219], v[104:119]\n\t"
    "v_cvt_pk_bf16_f32 v48, v48, v49\n\t"
    "v_cvt_pk_bf16_f32 v49, v50, v51\n\t"
    "v_cvt_pk_bf16_f32 v50, v52, v53\n\t"
    "v_cvt_pk_bf16_f32 v51, v54, v55\n\t"
    "v_exp_f32_e32 v192, v192\n\t"
    "v_mfma_f32_32x32x16_bf16 v[104:119], v[236:239], v[40:43], v[104:119]\n\t"
    "v_exp_f32_e32 v193, v193\n\t"
    "v_exp_f32_e32 v194, v194\n\t"
    "v_exp_f32_e32 v195, v195\n\t"
    "v_mfma_f32_16x16x32_bf16 v[172:175], v[24:27], v[48:51], v[172:175]\n\t"
    "v_cvt_pk_bf16_f32 v76, v192, v193\n\t"
    "v_cvt_pk_bf16_f32 v77, v194, v195\n\t"
    "v_mfma_f32_32x32x16_bf16 v[104:119], v[228:231], v[48:51], v[104:119]\n\t"
    "v_max3_u32 v36, v180, v181, v36\n\t"
    "v_max3_u32 v36, v182, v183, v36\n\t"
    "v_min3_u32 v37, v180, v181, v37\n\t"
    "v_min3_u32 v37, v182, v183, v37\n\t"
    "v_rcp_f32_e32 v180, v180\n\t"
    "v_mfma_f32_16x16x32_bf16 v[172:175], v[24:27], v[76:79], v[172:175]\n\t"
    "v_rcp_f32_e32 v181, v181\n\t"
    "v_mfma_f32_32x32x16_bf16 v[104:119], v[252:255], v[76:79], v[104:119]\n\t"
    "v_rcp_f32_e32 v182, v182\n\t"
    "v_rcp_f32_e32 v183, v183\n\t"
    "s_nop 3\n\t"
    "v_max_u32_e32 v36, v172, v36\n\t"
    "v_min_u32_e32 v37, v172, v37\n\t"
    "v_mfma_f32_16x16x32_bf16 v[232:235], v[204:207], v[248:251], 0\n\t"
    "v_rcp_f32_e32 v251, v172\n\t"
    "v_mfma_f32_16x16x32_bf16 v[232:235], v[200:203], v[240:243], v[232:235]\n\t"
    "s_nop 0\n\t"
    "v_fmac_f32_e32 v88, v104, v251\n\t"
    "v_fmac_f32_e32 v89, v105, v251\n\t"
    "v_mfma_f32_16x16x32_bf16 v[232:235], v[196:199], v[144:147], v[232:235]\n\t"
    "v_cvt_pk_bf16_f32 v88, v88, v89\n\t"
    "v_pk_max_i16 v88, v88, 0\n\t"
    "v_mfma_f32_16x16x32_bf16 v[232:235], v[188:191], v[140:143], v[232:235]\n\t"
    "v_fmac_f32_e32 v90, v106, v251\n\t"
    "v_fmac_f32_e32 v91, v107, v251\n\t"
    "v_mfma_f32_16x16x32_bf16 v[232:235], v[184:187], v[236:239], v[232:235]\n\t"
    "v_cvt_pk_bf16_f32 v89, v90, v91\n\t"
    "v_pk_max_i16 v89, v89, 0\n\t"
    "v_fmac_f32_e32 v92, v108, v251\n\t"
    "v_fmac_f32_e32 v93, v109, v251\n\t"
    "v_cvt_pk_bf16_f32 v90, v92, v93\n\t"
    "v_pk_max_i16 v90, v90, 0\n\t"
    "v_fmac_f32_e32 v94, v110, v251\n\t"
    "v_mfma_f32_16x16x32_bf16 v[232:235], v[244:247], v[228:231], v[232:235]\n\t"
    "v_fmac_f32_e32 v95, v111, v251\n\t"
    "v_cvt_pk_bf16_f32 v91, v94, v95\n\t"
    "v_pk_max_i16 v91, v91, 0\n\t"
    "ds_write_b128 v14, v[88:91] offset:0\n\t"
    "v_mfma_f32_16x16x32_bf16 v[232:235], v[224:227], v[252:255], v[232:235]\n\t"
    "v_fmac_f32_e32 v96, v112, v251\n\t"
    "v_fmac_f32_e32 v97, v113, v251\n\t"
    "v_cvt_pk_bf16_f32 v96, v96, v97\n\t"
    "v_pk_max_i16 v96, v96, 0\n\t"
    "v_fmac_f32_e32 v98, v114, v251\n\t"
    "v_fmac_f32_e32 v99, v115, v251\n\t"
    "v_cvt_pk_bf16_f32 v97, v98, v99\n\t"
    "v_pk_max_i16 v97, v97, 0\n\t"
    "v_fmac_f32_e32 v100, v116, v251\n\t"
    "v_fmac_f32_e32 v101, v117, v251\n\t"
    "v_cvt_pk_bf16_f32 v98, v100, v101\n\t"
    "v_pk_max_i16 v98, v98, 0\n\t"
    "v_fmac_f32_e32 v102, v118, v251\n\t"
    "v_fmac_f32_e32 v103, v119, v251\n\t"
    "v_cvt_pk_bf16_f32 v99, v102, v103\n\t"
    "v_pk_max_i16 v99, v99, 0\n\t"
    "ds_write_b128 v15, v[96:99] offset:0\n\t"
    "ds_read_b128 v[252:255], v16 offset:0\n\t"
    "ds_read_b128 v[248:251], v16 offset:1024\n\t"
    "v_fmac_f32_e32 v176, v232, v180\n\t"
    "v_fmac_f32_e32 v177, v233, v181\n\t"
    "v_fmac_f32_e32 v178, v234, v182\n\t"
    "v_fmac_f32_e32 v179, v235, v183\n\t"
    "v_cvt_pk_bf16_f32 v176, v176, v177\n\t"
    "v_cvt_pk_bf16_f32 v177, v178, v179\n\t"
    "s_setprio 2\n\t"
    "s_setprio 3\n\t"
    "v_cndmask_b32_e64 v176, v176, v177, %[hmask]\n\t"
    "v_pk_max_i16 v176, v176, 0\n\t"
    "buffer_store_short v176, v23, %[orsrc], 0 offen sc1\n\t"
    "buffer_store_short_d16_hi v176, v23, %[orsrc], 0 offen offset:128 sc1\n\t"
    "s_waitcnt lgkmcnt(1)\n\t"
    "buffer_store_dwordx4 v[252:255], v17, %[orsrc], s42 offen offset:0 sc1\n\t"
    "s_waitcnt lgkmcnt(0)\n\t"
    "buffer_store_dwordx4 v[248:251], v17, %[orsrc], s42 offen offset:2048 sc1\n\t"
    "s_nop 1"
    : "+{v[4:7]}"(ig0), "+{v[8:11]}"(ig1), "+{v[12:15]}"(ig2), "+{v[16:19]}"(ig3), "+{v[20:23]}"(ig4), "+{v[24:27]}"(ig5), "+{v[28:31]}"(ig6), "+{v[32:35]}"(ig7), "={v36}"(lmax), "={v37}"(lmin)
    : [orsrc] "s"(orr), [hmask] "s"(hmask), [role] "s"(role)
    : "memory", "scc", "s40", "s41", "s42", "s43", "s44", "s45", "v38", "v39", "v40", "v41", "v42", "v43", "v44", "v45", "v46", "v47", "v48", "v49", "v50", "v51", "v52", "v53", "v54", "v55", "v56", "v57", "v58", "v59", "v60", "v61", "v62", "v63", "v64", "v65", "v66", "v67", "v68", "v69", "v70", "v71", "v72", "v73", "v74", "v75", "v76", "v77", "v78", "v79", "v80", "v81", "v82", "v83", "v84", "v85", "v86", "v87", "v88", "v89", "v90", "v91", "v92", "v93", "v94", "v95", "v96", "v97", "v98", "v99", "v100", "v101", "v102", "v103", "v104", "v105", "v106", "v107", "v108", "v109", "v110", "v111", "v112", "v113", "v114", "v115", "v116", "v117", "v118", "v119", "v120", "v121", "v122", "v123", "v124", "v125", "v126", "v127", "v128", "v129", "v130", "v131", "v132", "v133", "v134", "v135", "v136", "v137", "v138", "v139", "v140", "v141", "v142", "v143", "v144", "v145", "v146", "v147", "v148", "v149", "v150", "v151", "v152", "v153", "v154", "v155", "v156", "v157", "v158", "v159", "v160", "v161", "v162", "v163", "v164", "v165", "v166", "v167", "v168", "v169", "v170", "v171", "v172", "v173", "v174", "v175", "v176", "v177", "v178", "v179", "v180", "v181", "v182", "v183", "v184", "v185", "v186", "v187", "v188", "v189", "v190", "v191", "v192", "v193", "v194", "v195", "v196", "v197", "v198", "v199", "v200", "v201", "v202", "v203", "v204", "v205", "v206", "v207", "v208", "v209", "v210", "v211", "v212", "v213", "v214", "v215", "v216", "v217", "v218", "v219", "v220", "v221", "v222", "v223", "v224", "v225", "v226", "v227", "v228", "v229", "v230", "v231", "v232", "v233", "v234", "v235", "v236", "v237", "v238", "v239", "v240", "v241", "v242", "v243", "v244", "v245", "v246", "v247", "v248", "v249", "v250", "v251", "v252", "v253", "v254", "v255");

    }
    if (__builtin_expect(__any(lmax > L_HI || lmin < L_LO), 0)) safe_unit(lds, ereg, head, outp);
}

extern "C" void kernel_launch(void* const* d_in, const int* in_sizes, int n_in,
                              void* d_out, int out_size, void* d_ws, size_t ws_size,
                              hipStream_t stream) {
    const int B = in_sizes[0] / (NF * 64);
    autoint_kernel<<<B / 4, 512, 0, stream>>>((const char*)d_in[0], (const char*)d_in[1], (const char*)d_in[2],
                                              (const char*)d_in[3], (const char*)d_in[4], (char*)d_out);
}
```
